```python
import torch

def _softmax(x, axis=-1):
    return torch.softmax(x, dim=axis)
def _gelu(x):
    return torch.nn.functional.gelu(x, approximate="tanh")
def _silu(x):
    return torch.nn.functional.silu(x)
def _sigmoid(x):
    return torch.sigmoid(x)
def _layernorm(x, axis=-1, eps=1e-5):
    m = x.mean(dim=axis, keepdim=True)
    v = x.var(dim=axis, keepdim=True, unbiased=False)
    return (x - m) / torch.sqrt(v + eps)
def _scatter_add(tgt, idx, src, axis=0):
    return tgt.clone().index_add_(axis, idx, src)
def _segment_sum(src, seg, n):
    out = torch.zeros((n,) + src.shape[1:], dtype=src.dtype)
    return out.index_add_(0, seg, src)
def _pad(x, pw):
    flat = tuple(v for pair in reversed(pw) for v in pair)
    return torch.nn.functional.pad(x, flat)

kernel_name = 'interleave_layers__ssm_conv_h3__linear_attn_block__prompt'

def setup_inputs():

    _w = (lambda a: (lambda t: t.to(torch.bfloat16) if t.is_floating_point() else t)(torch.as_tensor(a)))
    def _A_setup():
        B, S, D = 1, 2048, 1024
        ks = 4
        return {
            "q": _w((lambda *s: torch.randn(*s, dtype=torch.bfloat16) * 0.05)(B, S, D)),
            "k": _w((lambda *s: torch.randn(*s, dtype=torch.bfloat16) * 0.4)(B, S, D)),
            "v": _w((lambda *s: torch.randn(*s, dtype=torch.bfloat16) * 0.4)(B, S, D)),
            "shift_k": _w((lambda *s: torch.randn(*s, dtype=torch.bfloat16) * 0.4)(D, ks) * 0.5),
            "ssm_k": _w((lambda *s: torch.randn(*s, dtype=torch.bfloat16) * 0.4)(D, S) * (1.0 / S ** 0.5)),
            "ssm_D": _w((lambda *s: torch.randn(*s, dtype=torch.bfloat16) * 0.4)(D)),
        }
    _A_in = _A_setup()

    def _B_setup():
        B, S, D = 1, 2048, 1024
        return {
            "x": _w((lambda *s: torch.randn(*s, dtype=torch.bfloat16) * 0.05)(B, S, D)),
            "wqkv": _w((lambda *s: torch.randn(*s, dtype=torch.bfloat16) * 0.1)(D, 3 * D)),
            "wo": _w((lambda *s: torch.randn(*s, dtype=torch.bfloat16) * 0.05)(D, D)),
        }
    _B_in = _B_setup()

    return {
        "A_q": _A_in["q"],
        "A_k": _A_in["k"],
        "A_v": _A_in["v"],
        "A_shift_k": _A_in["shift_k"],
        "A_ssm_k": _A_in["ssm_k"],
        "A_ssm_D": _A_in["ssm_D"],
        "B_x": _B_in["x"],
        "B_wqkv": _B_in["wqkv"],
        "B_wo": _B_in["wo"],
    }

def reference(A_q, A_k, A_v, A_shift_k, A_ssm_k, A_ssm_D, B_x, B_wqkv, B_wo):
    def _A_ref(q, k, v, shift_k, ssm_k, ssm_D):
        B, S, D = q.shape
        ks = shift_k.shape[-1]
        k_pad = _pad(k, ((0, 0), (ks - 1, 0), (0, 0)))
        k_shift = torch.zeros_like(k)
        for t in range(ks):
            k_shift = k_shift + k_pad[:, t:t + S, :] * shift_k[:, ks - 1 - t][None, None, :]
        kv = k_shift * v
        L = 2 * S
        kv_pad = _pad(kv, ((0, 0), (0, S), (0, 0)))
        kf_pad = _pad(ssm_k, ((0, 0), (0, S)))
        kv_f = (lambda x, axes=(-2,-1): torch.fft.rfft2(x.float(), dim=axes))(kv_pad[:, None, :, :], axes=(1, 2))
        kf_f = (lambda x, axes=(-2,-1): torch.fft.rfft2(x.float(), dim=axes))(kf_pad[:, None, :], axes=(1, 2))
        kf_f = (lambda x, *d: x.permute(*d))(kf_f, 1, 2, 0)
        y_f = kv_f * kf_f[None, :, :, :]
        y = (lambda x, s, axes=(-2,-1): torch.fft.irfft2(x, s=s, dim=axes).to(torch.bfloat16))(y_f, (1, L), axes=(1, 2))[:, 0, :S, :]
        y = y + kv * ssm_D[None, None, :]
        return q * y

    def _B_ref(x, wqkv, wo):
        B, S, D = x.shape
        H = 32; Dh = D // H
        qkv = (lambda x, *d: x.permute(*d))((x @ wqkv).reshape(B, S, 3, H, Dh), 2, 0, 3, 1, 4)
        q, k, v = qkv[0], qkv[1], qkv[2]
        phi = lambda t: torch.where(t > 0, t + 1, torch.exp(t)) + 1e-6
        pq, pk = phi(q), phi(k)
        kv = torch.einsum("bhsd,bhse->bhde", pk, v)
        z = torch.einsum("bhsd->bhd", pk)
        num = torch.einsum("bhqd,bhde->bhqe", pq, kv)
        den = torch.einsum("bhqd,bhd->bhq", pq, z)[..., None] + 1e-6
        o = num / den
        return (lambda x, *d: x.permute(*d))(o, 0, 2, 1, 3).reshape(B, S, D) @ wo

    x = A_q
    x = x + _A_ref(_layernorm(x, axis=-1), A_k, A_v, A_shift_k, A_ssm_k, A_ssm_D)
    x = x + _B_ref(_layernorm(x, axis=-1), B_wqkv, B_wo)
    x = x + _A_ref(_layernorm(x, axis=-1), A_k, A_v, A_shift_k, A_ssm_k, A_ssm_D)
    x = x + _B_ref(_layernorm(x, axis=-1), B_wqkv, B_wo)
    return x
```

```cpp
#include <hip/hip_runtime.h>
#include <stdint.h>

typedef unsigned short u16;
typedef __bf16 bf16x2 __attribute__((ext_vector_type(2)));
typedef __bf16 bf16x4 __attribute__((ext_vector_type(4)));
typedef __bf16 bf16x8 __attribute__((ext_vector_type(8)));
typedef float f32x2 __attribute__((ext_vector_type(2)));
typedef float f32x4 __attribute__((ext_vector_type(4)));
typedef unsigned u32x2 __attribute__((ext_vector_type(2)));
typedef unsigned u32x4 __attribute__((ext_vector_type(4)));
typedef __amdgpu_buffer_rsrc_t rsrc_t;
#define LAS __attribute__((address_space(3)))
#define DI __device__ __forceinline__
typedef LAS char lchar;

#define ST_DECL
#define ST(i)
#define ST_END(k, dbg)
constexpr size_t WS_DBG = 64u << 20;

constexpr int S_ = 2048, D_ = 1024;
constexpr unsigned WS_UT = 0;
constexpr unsigned WS_X1 = 4u << 20;
constexpr unsigned WS_Y = 8u << 20;
constexpr unsigned WS_X3 = 12u << 20;
constexpr unsigned WS_PQ = 16u << 20;
constexpr unsigned WS_WQ = 20u << 20;
constexpr unsigned WS_WO = 26u << 20;
constexpr unsigned WS_XQ = 28u << 20;
constexpr unsigned WS_ST0 = 32u << 20;
constexpr unsigned WS_CS = WS_ST0 + 65536;
constexpr unsigned WS_KVF = WS_CS + 65536;
constexpr unsigned KVF_BYTES = 65536 + 2048;
constexpr unsigned WS_ZERO = WS_KVF + 2 * KVF_BYTES;
constexpr unsigned WS_EX = WS_ZERO;
constexpr unsigned WS_RS = WS_EX + 32 * 8 * 64 * 8;
constexpr unsigned ZERO_BYTES = WS_RS + 2 * 262144 - WS_ZERO;
static_assert(ZERO_BYTES == 256 * 160 * 16, "zeroed region");
constexpr unsigned WS_KVP = 34u << 20;
constexpr unsigned KVP_BLK = 2112 * 4;
constexpr unsigned WS_END = 40u << 20;

#define AUX_UT 16
#define AUX_XQ 0
#define AUX_X1 16
#define AUX_Z 16
#define AUX_W 16
#define AUX_PQ 16
#define AUX_X3 16

extern "C" __device__ float mul_nopk(float, float) __asm("llvm.amdgcn.fmul.legacy");
DI float bf_lo(unsigned u) { return __uint_as_float(u << 16); }
DI float bf_hi(unsigned u) { return __uint_as_float(u & 0xffff0000u); }
DI unsigned pk_bf(float lo, float hi) {
  f32x2 v = {lo, hi};
  return __builtin_bit_cast(unsigned, __builtin_convertvector(v, bf16x2));
}
template <int CTRL> DI float dpp_mov(float x) {
  return __int_as_float(__builtin_amdgcn_update_dpp(0, __float_as_int(x), CTRL, 0xf, 0xf, true));
}
DI float swap32_add(float x) {
  auto r = __builtin_amdgcn_permlane32_swap(__float_as_uint(x), __float_as_uint(x), false, false);
  return __uint_as_float(r[0]) + __uint_as_float(r[1]);
}
DI float swap16_add(float x) {
  auto r = __builtin_amdgcn_permlane16_swap(__float_as_uint(x), __float_as_uint(x), false, false);
  return __uint_as_float(r[0]) + __uint_as_float(r[1]);
}
DI float wave_sum(float x) {
  x += dpp_mov<0xB1>(x);
  x += dpp_mov<0x4E>(x);
  x += dpp_mov<0x141>(x);
  x += dpp_mov<0x140>(x);
  return swap32_add(swap16_add(x));
}
DI void lds_barrier() {
  asm volatile("" ::: "memory");
  __builtin_amdgcn_s_waitcnt(0xC07F);
  __builtin_amdgcn_s_barrier();
  asm volatile("" ::: "memory");
}
#define SB0() __builtin_amdgcn_sched_barrier(0)
#define SGB(mask, n) __builtin_amdgcn_sched_group_barrier(mask, n, 0)
#define WAIT_VM(n) asm volatile("s_waitcnt vmcnt(%0)" :: "n"(n) : "memory")
#define MFMA16(a, b, c) __builtin_amdgcn_mfma_f32_16x16x32_bf16((a), (b), (c), 0, 0, 0)
DI bf16x8 lds_b128(const char* p) { return *(const bf16x8*)p; }
DI bf16x8 lds_b128(const lchar* p) { return *(const LAS bf16x8*)p; }
DI bf16x8 lds_tr2(const char* p0, const char* p1) {
  bf16x4 a = __builtin_amdgcn_ds_read_tr16_b64_v4bf16((LAS bf16x4*)p0);
  bf16x4 b = __builtin_amdgcn_ds_read_tr16_b64_v4bf16((LAS bf16x4*)p1);
  return __builtin_shufflevector(a, b, 0, 1, 2, 3, 4, 5, 6, 7);
}
DI bf16x8 lds_2b64(const lchar* p0, const lchar* p1) {
  const u32x2 a = *(const volatile LAS u32x2*)p0, b = *(const volatile LAS u32x2*)p1;
  const u32x4 r = {a[0], a[1], b[0], b[1]};
  return __builtin_bit_cast(bf16x8, r);
}
DI f32x4 unpack4(u32x2 v) { return (f32x4){bf_lo(v[0]), bf_hi(v[0]), bf_lo(v[1]), bf_hi(v[1])}; }
DI bf16x8 pack8(f32x4 a, f32x4 b) {
  u32x4 p = {pk_bf(a[0], a[1]), pk_bf(a[2], a[3]), pk_bf(b[0], b[1]), pk_bf(b[2], b[3])};
  return __builtin_bit_cast(bf16x8, p);
}
DI rsrc_t make_rsrc(const void* p, unsigned bytes) { return __builtin_amdgcn_make_buffer_rsrc((void*)p, (short)0, (int)bytes, 0x00020000); }
#define BLDS_(o, p) "buffer_load_dwordx4 %1, %2, %3 offen offset:" #o p " lds\n\t"
#define BLDS_ASM(str) asm volatile("s_mov_b32 m0, %0\n\ts_nop 0\n\t" str :: "s"(lds_base), "v"(voff), "s"(rsrc), "s"(soff) : "memory")
#define BLDS_ALL(p)                                                                       \
  if constexpr (N == 1) BLDS_ASM(BLDS_(0, p));                                            \
  else if constexpr (N == 2) BLDS_ASM(BLDS_(0, p) BLDS_(1024, p));                        \
  else if constexpr (N == 3) BLDS_ASM(BLDS_(0, p) BLDS_(1024, p) BLDS_(2048, p));         \
  else BLDS_ASM(BLDS_(0, p) BLDS_(1024, p) BLDS_(2048, p) BLDS_(3072, p));
template <int N, int P = 0> DI void blds_n(rsrc_t rsrc, unsigned voff, unsigned soff, unsigned lds_base) {
  if constexpr (P == 0) { BLDS_ALL("") } else if constexpr (P == 1) { BLDS_ALL(" nt") } else { BLDS_ALL(" sc1") }
}
#define POL_A 0
#define POL_W 0
template <int AUX> DI void st8(rsrc_t r, unsigned voff, u32x2 v) { __builtin_amdgcn_raw_buffer_store_b64(v, r, voff, 0, AUX); }
template <int AUX> DI void st16(rsrc_t r, unsigned voff, u32x4 v) { __builtin_amdgcn_raw_buffer_store_b128(v, r, voff, 0, AUX); }

constexpr int NT = 2;
__global__ __launch_bounds__(512, 2) void k_prep(const u16* __restrict__ x0, const u16* __restrict__ kin,
                                                  const u16* __restrict__ vin, const u16* __restrict__ sk,
                                                  char* __restrict__ ws) {
  __shared__ __attribute__((aligned(16))) char smem[32768 + 2048];
  ST_DECL
  const int xcd = blockIdx.x & 7, slot = blockIdx.x >> 3, w = __builtin_amdgcn_readfirstlane(threadIdx.x >> 6), l = threadIdx.x & 63;
  const rsrc_t rw = make_rsrc(ws, WS_END);
  const int t = threadIdx.x & 255;
  if (w < 4) {
    const int ctile = 2 * xcd + (slot & 1), rtile = slot >> 1, c8 = l & 7;
    const int ch0 = ctile * 64 + c8 * 8, sl = w * 32 + (l >> 3) * 4, s0 = rtile * 128 + sl;
    const rsrc_t rk = make_rsrc(kin, S_ * D_ * 2), rv = make_rsrc(vin, S_ * D_ * 2);
    u32x4 kr[7], vr[4], wr[4];
    const u32x2 wl = *(const u32x2*)(sk + (ctile * 64 + l) * 4);
#pragma unroll
    for (int i = 0; i < 3; ++i)
      kr[i] = __builtin_amdgcn_raw_buffer_load_b128(rk, l < 8 ? (unsigned)((s0 - 3 + i) * D_ + ch0) * 2u : 0xfffffff0u, 0, NT);
#pragma unroll
    for (int i = 0; i < 4; ++i) kr[3 + i] = __builtin_amdgcn_raw_buffer_load_b128(rk, (unsigned)((s0 + i) * D_ + ch0) * 2u, 0, NT);
#pragma unroll
    for (int i = 0; i < 4; ++i) vr[i] = __builtin_amdgcn_raw_buffer_load_b128(rv, (unsigned)((s0 + i) * D_ + ch0) * 2u, 0, NT);
    SB0();
    ST(3);
    *(u32x2*)(smem + 32768 + w * 512 + l * 8) = wl;
#pragma unroll
    for (int i = 0; i < 4; ++i) wr[i] = *(const u32x4*)(smem + 32768 + w * 512 + c8 * 64 + i * 16);
#pragma unroll
    for (int i = 0; i < 3; ++i)
#pragma unroll
      for (int j = 0; j < 4; ++j) {
        const unsigned x = __builtin_amdgcn_ds_bpermute(4 * l - 32, kr[4 + i][j]);
        kr[i][j] = l < 8 ? kr[i][j] : x;
      }
    char* lp = smem + c8 * 2048 + (((124 - sl) * 2) ^ (c8 * 16));
#pragma unroll
    for (int j = 0; j < 4; ++j) {
      const f32x2 w0 = {bf_lo(wr[j][0]), bf_lo(wr[j][2])}, w1 = {bf_hi(wr[j][0]), bf_hi(wr[j][2])};
      const f32x2 w2 = {bf_lo(wr[j][1]), bf_lo(wr[j][3])}, w3 = {bf_hi(wr[j][1]), bf_hi(wr[j][3])};
      f32x2 kf[7], u[4];
#pragma unroll
      for (int i = 0; i < 7; ++i) kf[i] = (f32x2){bf_lo(kr[i][j]), bf_hi(kr[i][j])};
#pragma unroll
      for (int i = 0; i < 4; ++i)
        u[i] = (kf[i] * w3 + kf[i + 1] * w2 + kf[i + 2] * w1 + kf[i + 3] * w0) * (f32x2){bf_lo(vr[i][j]), bf_hi(vr[i][j])};
#pragma unroll
      for (int c = 0; c < 2; ++c) *(u32x2*)(lp + (2 * j + c) * 256) = (u32x2){pk_bf(u[3][c], u[2][c]), pk_bf(u[1][c], u[0][c])};
    }
    ST(4);
    lds_barrier();
#pragma unroll
    for (int i = 0; i < 4; ++i) {
      const int ch = 16 * i + (t >> 4);
      const u32x4 v = *(const u32x4*)(smem + ch * 256 + (((t & 15) ^ (ch >> 3)) * 16));
      st16<AUX_UT>(rw, WS_UT + (unsigned)((ctile * 64 + ch) * S_ + (S_ - 128 - rtile * 128)) * 2u + (t & 15) * 16, v);
    }
  } else {
    const int hw = w - 4, rgp = xcd * 32 + slot, row = rgp * 8 + 2 * hw;
    const rsrc_t rx = make_rsrc(x0, S_ * D_ * 2);
    u32x4 xr[2][2];
#pragma unroll
    for (int r = 0; r < 2; ++r)
#pragma unroll
      for (int i = 0; i < 2; ++i) xr[r][i] = __builtin_amdgcn_raw_buffer_load_b128(rx, (row + r) * (D_ * 2) + i * 1024 + l * 16, 0, NT);
    if (t < 160) st16<16>(rw, WS_ZERO + (rgp * 160 + t) * 16, (u32x4){0, 0, 0, 0});
    SB0();
    ST(3);
    char* xs = smem + 16384;
#pragma unroll
    for (int i = 0; i < 2; ++i)
#pragma unroll
      for (int q = 0; q < 2; ++q)
        *(u32x4*)(xs + (64 * i + l) * 128 + (((4 * q + hw) ^ (l & 7)) * 16)) =
            (u32x4){xr[0][i][2 * q], xr[0][i][2 * q + 1], xr[1][i][2 * q], xr[1][i][2 * q + 1]};
    ST(4);
    lds_barrier();
#pragma unroll
    for (int i = 0; i < 4; ++i) {
      const int e = 256 * i + t, pr = e >> 3;
      const u32x4 v = *(const u32x4*)(xs + pr * 128 + (((e & 7) ^ (pr & 7)) * 16));
      st16<AUX_XQ>(rw, WS_XQ + (unsigned)(e >> 2) * (S_ * 8) + rgp * 64 + (e & 3) * 16, v);
    }
    SB0();
    float mean[2], var[2];
#pragma unroll
    for (int r = 0; r < 2; ++r) {
      float s = 0.f;
#pragma unroll
      for (int i = 0; i < 8; ++i) s += bf_lo(xr[r][i >> 2][i & 3]) + bf_hi(xr[r][i >> 2][i & 3]);
      mean[r] = wave_sum(s) * (1.f / D_);
    }
#pragma unroll
    for (int r = 0; r < 2; ++r) {
      float s = 0.f;
#pragma unroll
      for (int i = 0; i < 8; ++i) {
        const float a = bf_lo(xr[r][i >> 2][i & 3]) - mean[r], c = bf_hi(xr[r][i >> 2][i & 3]) - mean[r];
        s += a * a + c * c;
      }
      var[r] = wave_sum(s) * (1.f / D_);
    }
    if (l == 0) {
      const float r0 = __builtin_amdgcn_rsqf(var[0] + 1e-5f), r1 = __builtin_amdgcn_rsqf(var[1] + 1e-5f);
      const f32x4 o = {r0, -mean[0] * r0, r1, -mean[1] * r1};
      *(f32x4*)(ws + WS_ST0 + (size_t)row * 8) = o;
    }
  }
  ST(2);
  ST_END(0, ws + WS_DBG);
}

constexpr int CV_H = 4224, CV_U = 4576, CV_CH = 2 * CV_H + 2 * CV_U;
constexpr int CV_YP = 4096 + 64;
constexpr int CV_Y = 4 * CV_CH;
constexpr int CV_CS = CV_Y + 4 * CV_YP;
constexpr int CV_W = CV_CS + 1024;
constexpr int CV_LDS = CV_W + 32768;
#define CV_SPLIT 8

DI void mfma_ip(f32x4& acc, bf16x8 a, bf16x8 b) { asm volatile("v_mfma_f32_16x16x32_bf16 %0, %1, %2, %0" : "+v"(acc) : "v"(a), "v"(b)); }
template <int N0, int N1> DI void conv_tiles(const lchar* aa, const lchar* ba, bf16x8 dfrag, lchar* yb) {
  u32x2 A[2][N1][2];
  bf16x8 B[2][N1];
  f32x4 acc[N1 - N0];
  constexpr int NM = (N1 * (N1 + 1) - N0 * (N0 + 1)) / 2, NR = 3 * N1;
  constexpr int G2 = NR > NM - 4 ? NR - (NM - 4) : 0;
  auto rd = [&](int s, int o, int r) {
    const int p = r / 3, k = r % 3;
    if (k == 0) B[s][N1 - 1 - p] = *(const volatile LAS bf16x8*)(ba + 512 * (8 - N1 + p) + o);
    else A[s][p][k - 1] = *(const volatile LAS u32x2*)(aa + 512 * p + o + 8 * (k - 1));
  };
  auto step = [&](int s, int o, bool more) {
    int m = 0, r = 0;
#pragma unroll
    for (int d = 0; d < N1; ++d)
#pragma unroll
      for (int p = 0; p <= d; ++p) {
        const int dl = N1 - 1 - d, n = p + dl;
        if (n >= N0) {
          const u32x4 af = {A[s][p][0][0], A[s][p][0][1], A[s][p][1][0], A[s][p][1][1]};
          mfma_ip(acc[n - N0], __builtin_bit_cast(bf16x8, af), B[s][dl]);
          if (more && r < NR) rd(s ^ 1, o, r++);
          if (more && m < G2) rd(s ^ 1, o, r++);
          if (more && m == NM - 2) __builtin_amdgcn_s_waitcnt(0xC07F);
          ++m;
        }
      }
  };
#pragma unroll
  for (int r = 0; r < NR; ++r) rd(0, 0, r);
#pragma unroll
  for (int n = N1 - 1; n >= N0; --n) { acc[n - N0] = (f32x4){0.f, 0.f, 0.f, 0.f}; asm volatile("" : "+v"(acc[n - N0])); }
  asm volatile("s_nop 1");
#pragma unroll
  for (int n = N1 - 1; n >= N0; --n) mfma_ip(acc[n - N0], dfrag, B[0][n]);
#pragma unroll 1
  for (int it = 0; it < 3; ++it) {
    step(0, 64, true);
    step(1, 128, true);
    aa += 128; ba += 128;
  }
  step(0, 64, true);
  step(1, 0, false);
  asm volatile("s_nop 7\n\ts_nop 3");
  SB0();
#pragma unroll
  for (int n = N0; n < N1; ++n)
    *(LAS u32x2*)(yb + n * 512) = (u32x2){pk_bf(acc[n - N0][0], acc[n - N0][1]), pk_bf(acc[n - N0][2], acc[n - N0][3])};
}

__global__ __launch_bounds__(512, 2) void k_conv(const u16* __restrict__ hk, const u16* __restrict__ dvec,
                                                  const u16* __restrict__ wqkv, const u16* __restrict__ wo,
                                                  char* __restrict__ ws) {
  extern __shared__ __attribute__((aligned(16))) char smem[];
  ST_DECL
  const int b = blockIdx.x, w = __builtin_amdgcn_readfirstlane(threadIdx.x >> 6), l = threadIdx.x & 63;
  const int quad = (b & 7) * 32 + (b >> 3), ch = quad * 4 + (w & 3);
  char* cb = smem + (w & 3) * CV_CH;
  const u32x4 z4 = {0, 0, 0, 0};
  u32x4 c[4];
  unsigned nx[4];
  {
    const rsrc_t rs = make_rsrc((w < 4 ? (const char*)hk : ws + WS_UT) + (size_t)ch * (S_ * 2), S_ * 2);
#pragma unroll
    for (int i = 0; i < 4; ++i)
      asm volatile("buffer_load_dwordx4 %0, %1, %2, 0 offen offset:%3 nt" : "=v"(c[i]) : "v"(l * 16), "s"(rs), "i"(i * 1024) : "memory");
  }
  const unsigned dv = (((const unsigned*)dvec)[ch >> 1] >> ((ch & 1) * 16)) & 0xffffu;
  const bool isq = b < 192;
  const int kt = (b & 3) * 4 + (w & 3), cgp = (isq ? b : b - 192) >> 2;
  asm volatile("s_waitcnt vmcnt(0)" : "+v"(c[0]), "+v"(c[1]), "+v"(c[2]), "+v"(c[3]) :: "memory");
  SB0();
#pragma unroll
  for (int i = 0; i < 4; ++i)
    nx[i] = __builtin_amdgcn_update_dpp(i < 3 ? __builtin_amdgcn_readfirstlane(c[i < 3 ? i + 1 : 0][0]) : 0, c[i][0], 0x130, 0xf, 0xf, false);
  if (w < 4) {
    if (l < 4) *(u32x4*)(cb + l * 16) = z4;
    if (l < 3) *(u32x4*)(cb + CV_H + l * 16) = z4;
    if (l == 0) *(u32x4*)(cb + CV_H + 48) = (u32x4){0, 0, 0, c[0][0]};
#pragma unroll
    for (int i = 0; i < 4; ++i) {
      const int cc = l + 64 * i;
      *(u32x4*)(cb + 64 + cc * 16) = c[i];
      *(u32x4*)(cb + CV_H + 64 + cc * 16) = (u32x4){c[i][1], c[i][2], c[i][3], nx[i]};
    }
    const int r = l & 15, g = l >> 4;
    const lchar* aa = (lchar*)cb + ((r & 1) ? 4 : CV_H) + 4 * r + 16 * g;
    const lchar* ba = (lchar*)cb + 2 * CV_H + ((r >> 3) ? 0 : CV_U) + 448 - 64 * (r & 7) + 16 * g;
    u32x4 dfr;
    const int kk = 30 - 2 * r - 8 * g;
#pragma unroll
    for (int i = 0; i < 4; ++i) dfr[i] = (kk == 2 * i) ? dv : 0u;
    lchar* yb = (lchar*)smem + CV_Y + w * CV_YP + r * 32 + ((g ^ (r >> 2)) & 3) * 8;
    ST(2);
    lds_barrier();
    ST(3);
    conv_tiles<0, CV_SPLIT>(aa, ba, __builtin_bit_cast(bf16x8, dfr), yb);
    ST(4);
    lds_barrier();
    if (CV_SPLIT < 8) {
      conv_tiles<CV_SPLIT < 8 ? CV_SPLIT : 0, 8>(aa, ba, __builtin_bit_cast(bf16x8, dfr), yb);
      ST(5);
      lds_barrier();
    }
  } else {
    const int t = threadIdx.x - 256, hw = w - 4;
    const rsrc_t rw = make_rsrc(ws, WS_END);
    {
      char* ub = cb + 2 * CV_H;
      if (l < 30) { *(u32x4*)(ub + 4096 + l * 16) = z4; *(u32x4*)(ub + CV_U + 4096 + l * 16) = z4; }
#pragma unroll
      for (int i = 0; i < 4; ++i) {
        const int cc = l + 64 * i;
        *(u32x4*)(ub + cc * 16) = c[i];
        *(u32x4*)(ub + CV_U + cc * 16) =
            (u32x4){__builtin_amdgcn_alignbyte(c[i][1], c[i][0], 2), __builtin_amdgcn_alignbyte(c[i][2], c[i][1], 2),
                    __builtin_amdgcn_alignbyte(c[i][3], c[i][2], 2), __builtin_amdgcn_alignbyte(nx[i], c[i][3], 2)};
      }
    }
    SB0();
    ST(2);
    lds_barrier();
    ST(3);
    {
    const unsigned pitch = isq ? 6144 : 2048;
    const rsrc_t rwt = make_rsrc(isq ? wqkv : wo, pitch * 1024);
    const unsigned vo = (kt * 64 + (l >> 3)) * pitch + cgp * 128 + (((l & 7) ^ ((l >> 3) & 6)) * 16);
#pragma unroll
    for (int i = 0; i < 8; ++i) blds_n<1, 1>(rwt, vo, i * 8 * pitch, (unsigned)(size_t)(LAS char*)smem + CV_W + (w - 4) * 8192 + i * 1024);
    }
    u32x4 xq[4];
    f32x4 st[4];
#pragma unroll
    for (int i = 0; i < 4; ++i) {
      xq[i] = *(const u32x4*)(ws + WS_XQ + (size_t)quad * (S_ * 8) + (4 * i + hw) * 1024 + l * 16);
      st[i] = *(const f32x4*)(ws + WS_ST0 + (4 * i + hw) * 1024 + l * 16);
    }
    SB0();
    WAIT_VM(8);
    bf16x8 fg[8];
    {
      char* wb = smem + CV_W + hw * 8192;
      const int g = l >> 4, q4 = (l >> 2) & 3;
      const int rd = (4 * g + q4) * 128 + (l & 3) * 8, fr = ((2 * g + (q4 >> 1)) & 3) * 32;
      const unsigned o = (isq ? WS_WQ + (unsigned)((cgp & 15) * 16 + kt) * 24576u + (cgp >> 4) * 4096u
                              : WS_WO + (unsigned)(cgp * 16 + kt) * 8192u) + l * 16;
      const unsigned o2 = isq ? 12288u : 4096u;
#pragma unroll
      for (int i = 0; i < 8; ++i) {
        const char* p = wb + rd + (i >> 2) * 4096 + (((i & 3) * 32) ^ fr);
        fg[i] = lds_tr2(p, p + 2048);
        st16<AUX_W>(rw, o + (i >> 2) * o2 + (i & 3) * 1024, __builtin_bit_cast(u32x4, fg[i]));
      }
    }
#pragma unroll
    for (int i = 0; i < 3; ++i) st16<AUX_Z>(rw, t + 256 * i < 528 ? WS_KVP + (b * 528 + t + 256 * i) * 16 : 0xfffffff0u, z4);
    f32x4 xf[4][2], xn[4][2];
#pragma unroll
    for (int i = 0; i < 4; ++i)
#pragma unroll
      for (int r = 0; r < 2; ++r) {
        xf[i][r] = unpack4((u32x2){xq[i][2 * r], xq[i][2 * r + 1]});
#pragma unroll
        for (int e = 0; e < 4; ++e)
          asm("v_fma_f32 %0, %1, %2, %3" : "=v"(xn[i][r][e]) : "v"(xf[i][r][e]), "v"(st[i][2 * r]), "v"(st[i][2 * r + 1]));
        asm volatile("" : "+v"(xf[i][r]), "+v"(xn[i][r]));
      }
    const int ya = 4 * (hw & 1) + (l >> 4);
    const char* yp = smem + CV_Y + ((l >> 2) & 3) * CV_YP + (hw >> 1) * 512 + ya * 32;
    const int y0 = ((l ^ (ya >> 2)) & 3) * 8, y1 = 256 + ((l ^ (2 + (ya >> 2))) & 3) * 8;
    const unsigned go = (unsigned)hw * 262144 + quad * 1024 + ((l * 16) ^ ((quad & 1) * 128));
    auto fin = [&](int i) {
      const u32x4 y = __builtin_bit_cast(u32x4, lds_tr2(yp + i * 1024 + y0, yp + i * 1024 + y1));
      st16<AUX_X1>(rw, WS_Y + go + i * 1048576, y);
      const f32x4 o0 = xf[i][0] + xn[i][0] * unpack4((u32x2){y[0], y[1]}), o1 = xf[i][1] + xn[i][1] * unpack4((u32x2){y[2], y[3]});
      st16<AUX_X1>(rw, WS_X1 + go + i * 1048576, __builtin_bit_cast(u32x4, pack8(o0, o1)));
    };
    ST(4);
    lds_barrier();
#pragma unroll
    for (int i = 0; i < CV_SPLIT / 2; ++i) fin(i);
    if (CV_SPLIT < 8) {
      ST(5);
      lds_barrier();
#pragma unroll
      for (int i = CV_SPLIT / 2; i < 4; ++i) fin(i);
    }
    ST(7);
    if (isq) {
      const u32x4 one4 = {0x3f803f80u, 0x3f803f80u, 0x3f803f80u, 0x3f803f80u};
#pragma unroll
      for (int i = 0; i < 4; ++i) {
        const f32x4 cs = MFMA16(__builtin_bit_cast(bf16x8, one4), fg[4 + i],
                                MFMA16(__builtin_bit_cast(bf16x8, one4), fg[i], ((f32x4){0.f, 0.f, 0.f, 0.f})));
        if (l < 16) ((float*)(smem + CV_CS))[hw * 64 + i * 16 + l] = cs[0];
      }
      lds_barrier();
      const float* red = (const float*)(smem + CV_CS) + l;
      if (w == 4) ((float*)(ws + WS_CS))[(b & 3) * 3072 + cgp * 64 + l] = red[0] + red[64] + red[128] + red[192];
    }
  }
  ST(6);
  ST_END(1, ws + WS_DBG);
}

#define QK_D 2
#define QK_EXP 0
#define QK_SLP0 8
constexpr int QK_STAGE = 40960, QK_NSL = 3;
constexpr int QK_RS = QK_NSL * QK_STAGE;
constexpr int QK_KV = QK_RS + 2048;
constexpr int QK_LDS = QK_KV + 24576;
#define QK_P 4
constexpr float LOG2E = 1.44269504088896f, LN2 = 0.693147180559945f;

DI f32x4 phi4(f32x4 u) {
  f32x4 e, m;
#pragma unroll
  for (int i = 0; i < 4; ++i) {
    e[i] = __builtin_amdgcn_fmed3f(__builtin_amdgcn_exp2f(u[i]), 0.f, 1.f);
    m[i] = fmaxf(u[i], 1e-6f * LOG2E);
  }
  return m * LN2 + e;
}

__global__ __launch_bounds__(512, 2) void k_qkv(char* __restrict__ ws, unsigned xoff, unsigned layer) {
  extern __shared__ __attribute__((aligned(16))) char smem[];
  ST_DECL
  const int b = blockIdx.x, w = __builtin_amdgcn_readfirstlane(threadIdx.x >> 6), l = threadIdx.x & 63;
  const int xcd = b & 7, slot = b >> 3;
  const int panel = 8 * (xcd & 1) + (slot & 7), ct = 4 * (xcd >> 1) + (slot >> 3);
  const rsrc_t rw = make_rsrc(ws, WS_END);
  float* s_rs = (float*)(smem + QK_RS);
  float* s_cs = s_rs + 256;
  float* s_kv = (float*)(smem + QK_KV);
  const unsigned rsoff = WS_RS + layer * 262144 + panel * 16384;
  if (w >= 4) {
    const int lw = w - 4;
    const unsigned lbase = (unsigned)(size_t)(LAS char*)smem;
    const unsigned va = xoff + panel * 262144 + lw * 2048 + l * 16;
    const unsigned vb = WS_WQ + ct * (32 * 12288) + lw * 3072 + l * 16;
    auto issue = [&](int t, int sl, int h) {
      const int o = t < QK_P ? t : t - 1;
      const int ks = 2 * (t == QK_P ? ct : (o < ct ? o : o + 1)) + h;
      const unsigned sb = lbase + sl * QK_STAGE;
      blds_n<2>(rw, va, ks * 8192, sb + (4 * h + lw) * 2048);
      blds_n<3>(rw, vb, ks * 12288, sb + 16384 + h * 12288 + lw * 3072);
    };
#define LBAR() __builtin_amdgcn_s_barrier()
    issue(0, 0, 0); issue(0, 0, 1); issue(1, 1, 0);
    const int t = threadIdx.x - 256, c = l & 15, g = l >> 4;
    unsigned ro = rsoff + 4 * g * 1024 + (32 * lw + 2 * c) * 8;
    unsigned co = t < 192 ? WS_CS + (g * 3072 + lw * 1024 + ct * 64 + c * 4) * 4 : 0xfffffff0u;
    LAS float* wrs = (LAS float*)s_rs + (g >> 1) * 128 + 32 * lw + 2 * c + (g & 1);
    LAS float* wcs = (LAS float*)s_cs + lw * 64 + c * 4 + g;
    asm volatile("" : "+v"(ro), "+v"(co), "+v"(wrs), "+v"(wcs));
    ST(2);
    WAIT_VM(5);
    LBAR();
    issue(1, 1, 1);
    int sl = 2;
    for (int u = 0; u < 13; ++u) {
      issue(u + 2, sl, 0); issue(u + 2, sl, 1);
      sl = sl == 2 ? 0 : sl + 1;
      WAIT_VM(10);
      LBAR();
    }
    issue(15, 0, 0); issue(15, 0, 1);
    u32x4 xr[4];
#pragma unroll
    for (int i = 0; i < 4; ++i) xr[i] = __builtin_amdgcn_raw_buffer_load_b128(rw, ro, i * 1024, 16);
    f32x4 c0 = __builtin_bit_cast(f32x4, __builtin_amdgcn_raw_buffer_load_b128(rw, co, 0, 0));
    SB0();
    WAIT_VM(15);
    LBAR();
    SB0();
    __builtin_amdgcn_s_setprio(2);
#pragma unroll 1
    for (unsigned spin = 0; spin < (1u << 17); ++spin) {
      if (__all(xr[0][1] & xr[0][3] & xr[1][1] & xr[1][3] & xr[2][1] & xr[2][3] & xr[3][1] & xr[3][3] & 1)) break;
      __builtin_amdgcn_s_sleep(1);
#pragma unroll
      for (int i = 0; i < 4; ++i) xr[i] = __builtin_amdgcn_raw_buffer_load_b128(rw, ro, i * 1024, 16);
      asm volatile("" ::: "memory");
    }
    __builtin_amdgcn_s_waitcnt(0x0F70);
    SB0();
    __builtin_amdgcn_s_barrier();
    SB0();
    {
      float p[4];
#pragma unroll
      for (int e = 0; e < 4; ++e) {
        float a = __uint_as_float(xr[0][e]) + __uint_as_float(xr[1][e]), d = __uint_as_float(xr[2][e]) + __uint_as_float(xr[3][e]);
        asm("" : "+v"(a));
        p[e] = a + d;
        asm("" : "+v"(p[e]));
      }
      auto r0 = __builtin_amdgcn_permlane16_swap(__float_as_uint(p[0]), __float_as_uint(p[2]), false, false);
      auto r1 = __builtin_amdgcn_permlane16_swap(__float_as_uint(p[1]), __float_as_uint(p[3]), false, false);
      const float mean = swap32_add(__uint_as_float(r0[0]) + __uint_as_float(r0[1])) * (1.f / D_);
      const float ex2 = swap32_add(__uint_as_float(r1[0]) + __uint_as_float(r1[1])) * (1.f / D_);
      const float rs = __builtin_amdgcn_rsqf(fmaxf(ex2 - mean * mean, 0.f) + 1e-5f) * LOG2E;
      *wrs = (g >> 1) ? -mean * rs : rs;
      auto q0 = __builtin_amdgcn_permlane16_swap(__float_as_uint(c0[0]), __float_as_uint(c0[1]), false, false);
      auto q1 = __builtin_amdgcn_permlane16_swap(__float_as_uint(c0[2]), __float_as_uint(c0[3]), false, false);
      auto q2 = __builtin_amdgcn_permlane32_swap(__float_as_uint(__uint_as_float(q0[0]) + __uint_as_float(q0[1])),
                                                 __float_as_uint(__uint_as_float(q1[0]) + __uint_as_float(q1[1])), false, false);
      *wcs = __uint_as_float(q2[0]) + __uint_as_float(q2[1]);
    }
    ST(3);
    lds_barrier();
    ST(4);
    lds_barrier();
    ST(5);
    const unsigned pb = WS_KVP + ct * (16 * KVP_BLK);
#pragma unroll
    for (int i = 0; i < 3; ++i) {
      const int e = t + 256 * i;
      u32x4 v = __builtin_bit_cast(u32x4, *(const f32x4*)(s_kv + e * 4) + *(const f32x4*)(s_kv + 2112 + e * 4));
      v[1] |= 1u; v[3] |= 1u;
      st16<16>(rw, e < 528 ? pb + panel * KVP_BLK + e * 16 : 0xfffffff0u, v);
    }
    ST(6);
    char* kvf = ws + WS_KVF + layer * KVF_BYTES;
    __builtin_amdgcn_s_sleep(QK_SLP0);
    if (lw == 0) {
      const unsigned go = pb + (8 * (l >> 5)) * KVP_BLK + panel * 512 + (l & 31) * 16;
      u32x4 v[8];
      for (unsigned spin = 0; spin < (1u << 17); ++spin) {
        unsigned ok = 1;
#pragma unroll
        for (int i = 0; i < 8; ++i) {
          v[i] = __builtin_amdgcn_raw_buffer_load_b128(rw, go + i * KVP_BLK, 0, 16);
          ok &= v[i][1] & v[i][3];
        }
        if (__all(ok)) break;
        asm volatile("" ::: "memory");
      }
      f32x4 a = __builtin_bit_cast(f32x4, v[0]);
#pragma unroll
      for (int i = 1; i < 8; ++i) a += __builtin_bit_cast(f32x4, v[i]);
#pragma unroll
      for (int e = 0; e < 4; ++e) a[e] = swap32_add(a[e]);
      if (l < 32) *(u32x2*)(kvf + ct * 4096 + panel * 256 + l * 8) = (u32x2){pk_bf(a[0], a[1]), pk_bf(a[2], a[3])};
    } else if (lw == 1 && panel < 8) {
      const unsigned go = pb + ((l >> 1) & 15) * KVP_BLK + 8192 + panel * 32 + (l & 1) * 16;
      u32x4 v;
      for (unsigned spin = 0; spin < (1u << 17); ++spin) {
        v = __builtin_amdgcn_raw_buffer_load_b128(rw, go, 0, 16);
        if (__all(v[1] & v[3] & 1)) break;
        asm volatile("" ::: "memory");
      }
      f32x4 a = __builtin_bit_cast(f32x4, v);
#pragma unroll
      for (int e = 0; e < 4; ++e) {
        a[e] += dpp_mov<0x4E>(a[e]);
        a[e] += dpp_mov<0x124>(a[e]);
        a[e] += dpp_mov<0x128>(a[e]);
        a[e] = swap16_add(a[e]);
      }
      if (l < 2) *(u32x2*)(kvf + 65536 + ct * 128 + panel * 16 + l * 8) = (u32x2){pk_bf(a[0], a[1]), pk_bf(a[2], a[3])};
    }
    ST(7);
  } else {
    const int wr = w >> 1, wh = w & 1, c = l & 15, g = l >> 4;
    f32x4 acc[6][4];
#pragma unroll
    for (int j = 0; j < 6; ++j)
#pragma unroll
      for (int i = 0; i < 4; ++i) { acc[j][i] = (f32x4){0.f, 0.f, 0.f, 0.f}; asm volatile("" : "+v"(acc[j][i])); }
    const lchar* qa = (lchar*)smem + g * 1024 + (64 * wr + c) * 8 + (g & 1) * 128;
    const lchar* qa2 = qa + ((g & 1) ? -128 : 128);
    const lchar* qb = (lchar*)smem + 16384 + wh * 2048 + l * 16;
    bf16x8 fa[2][4], fb[2][6];
    auto ldf = [&](int buf, int h) {
      fa[buf][0] = lds_2b64(qa + h * 8192, qa + h * 8192 + 4096);
      fb[buf][0] = lds_b128(qb + h * 12288);
      fa[buf][1] = lds_2b64(qa2 + h * 8192, qa2 + h * 8192 + 4096);
      fa[buf][2] = lds_2b64(qa + h * 8192 + 256, qa + h * 8192 + 256 + 4096);
      fa[buf][3] = lds_2b64(qa2 + h * 8192 + 256, qa2 + h * 8192 + 256 + 4096);
#pragma unroll
      for (int j = 1; j < 6; ++j) fb[buf][j] = lds_b128(qb + h * 12288 + (j >> 1) * 4096 + (j & 1) * 1024);
    };
    auto mma = [&](int buf, int m0, int m1) {
#pragma unroll
      for (int m = m0; m < m1; ++m) {
        const int j = m >> 2, i = m & 3;
        acc[j][i] = j < 2 ? MFMA16(fb[buf][j], fa[buf][i], acc[j][i])
                          : MFMA16(fa[buf][i], fb[buf][j], acc[j][i]);
      }
    };
    u32x4 one4 = {0x3f803f80u, 0x3f803f80u, 0x3f803f80u, 0x3f803f80u};
    const f32x4 zero4 = {0.f, 0.f, 0.f, 0.f};
    unsigned po = (c >> 2) == g ? rsoff + ct * 1024 + (64 * wr + 32 * wh + c) * 8 : 0xfffffff0u;
    asm volatile("" : "+v"(qa), "+v"(qa2), "+v"(qb), "+v"(one4), "+v"(po));
    int sl = 0;
    auto tile = [&](bool stats) {
      ldf(1, 1);
      mma(0, 0, 16);
#pragma unroll
      for (int i = 0; i < 14; ++i) { SGB(0x8, 1); SGB(0x100, 1); }
      SGB(0x8, 2);
      SB0();
      const int d = sl == 2 ? -2 * QK_STAGE : QK_STAGE;
      sl = sl == 2 ? 0 : sl + 1;
      qa += d; qa2 += d; qb += d;
      asm volatile("" : "+v"(qa), "+v"(qa2), "+v"(qb));
      mma(0, 16, 24);
      SB0();
      f32x4 m1[2], m2[2];
      if (stats) {
        if (wh) {
#pragma unroll
          for (int i = 0; i < 2; ++i) {
            m1[i] = MFMA16(__builtin_bit_cast(bf16x8, one4), fa[1][2 + i], MFMA16(__builtin_bit_cast(bf16x8, one4), fa[0][2 + i], zero4));
            m2[i] = MFMA16(fa[1][2 + i], fa[1][2 + i], MFMA16(fa[0][2 + i], fa[0][2 + i], zero4));
          }
        } else {
#pragma unroll
          for (int i = 0; i < 2; ++i) {
            m1[i] = MFMA16(__builtin_bit_cast(bf16x8, one4), fa[1][i], MFMA16(__builtin_bit_cast(bf16x8, one4), fa[0][i], zero4));
            m2[i] = MFMA16(fa[1][i], fa[1][i], MFMA16(fa[0][i], fa[0][i], zero4));
          }
        }
        SB0();
      }
      mma(1, 0, 1);
      SB0();
      lds_barrier();
      SB0();
      ldf(0, 0);
      mma(1, 1, 24);
      if (stats) {
#pragma unroll
        for (int i = 0; i < 2; ++i) {
          float e0 = m2[i][0], e1 = m2[i][1], e2 = m2[i][2], e3 = m2[i][3];
          asm("" : "+v"(e0), "+v"(e1), "+v"(e2), "+v"(e3));
          const float a1 = m1[i][0], a2 = (l & 2) ? ((l & 1) ? e3 : e2) : ((l & 1) ? e1 : e0);
          __builtin_amdgcn_raw_buffer_store_b64((u32x2){__float_as_uint(a1), __float_as_uint(a2) | 1u}, rw, po, i * 128, 16);
        }
#pragma unroll
        for (int i = 0; i < 14; ++i) { SGB(0x8, 1); SGB(0x100, 1); SGB(0x2, 1); }
#pragma unroll
        for (int i = 0; i < 2; ++i) { SGB(0x8, 1); SGB(0x2, 2); }
        SGB(0x8, 2); SGB(0x40, 1); SGB(0x8, 2); SGB(0x40, 1); SGB(0x8, 3);
      } else {
#pragma unroll
        for (int i = 0; i < 14; ++i) { SGB(0x8, 1); SGB(0x100, 1); }
        SGB(0x8, 9);
      }
      SB0();
    };
    ST(2);
    lds_barrier();
    ST(3);
    ldf(0, 0);
    SB0();
#pragma unroll 1
    for (int t = 0; t < QK_P; ++t) tile(false);
    tile(true);
#pragma unroll 1
    for (int t = QK_P + 1; t < 15; ++t) tile(false);
    ldf(1, 1);
    mma(0, 0, 24);
#pragma unroll
    for (int i = 0; i < 14; ++i) { SGB(0x8, 1); SGB(0x100, 1); }
    SGB(0x8, 10);
    SB0();
    mma(1, 8, 20);
    SB0();
    lds_barrier();
    SB0();
    mma(1, 20, 24);
    mma(1, 0, 8);
    ST(4);
    {
      f32x4 kv[2][2], zz[2];
      float csk[2], csv[2];
#pragma unroll
      for (int a = 0; a < 2; ++a) {
        kv[a][0] = kv[a][1] = zz[a] = (f32x4){0.f, 0.f, 0.f, 0.f};
        csk[a] = s_cs[64 + wh * 32 + 16 * a + c];
        csv[a] = s_cs[128 + wh * 32 + 16 * a + c];
      }
#pragma unroll
      for (int ip = 0; ip < 2; ++ip) {
        f32x4 rs[2], nm[2];
#pragma unroll
        for (int h = 0; h < 2; ++h) {
          rs[h] = *(const f32x4*)(s_rs + 64 * wr + 32 * ip + 16 * h + 4 * g);
          nm[h] = *(const f32x4*)(s_rs + 128 + 64 * wr + 32 * ip + 16 * h + 4 * g);
        }
        bf16x8 fk[2], fv[2];
#pragma unroll
        for (int a = 0; a < 2; ++a) {
          fk[a] = pack8(phi4(acc[2 + a][2 * ip] * rs[0] + (nm[0] * csk[a] + 1e-6f * LOG2E)),
                        phi4(acc[2 + a][2 * ip + 1] * rs[1] + (nm[1] * csk[a] + 1e-6f * LOG2E)));
        }
#pragma unroll
        for (int h = 0; h < 2; ++h) { rs[h] *= LN2; nm[h] *= LN2; }
#pragma unroll
        for (int a = 0; a < 2; ++a)
          fv[a] = pack8(acc[4 + a][2 * ip] * rs[0] + nm[0] * csv[a], acc[4 + a][2 * ip + 1] * rs[1] + nm[1] * csv[a]);
#pragma unroll
        for (int a = 0; a < 2; ++a) {
          kv[a][0] = MFMA16(fk[a], fv[0], kv[a][0]);
          kv[a][1] = MFMA16(fk[a], fv[1], kv[a][1]);
          zz[a] = MFMA16(fk[a], __builtin_bit_cast(bf16x8, one4), zz[a]);
        }
      }
      float* dst = s_kv + wr * 2112;
#pragma unroll
      for (int qd = 0; qd < 4; ++qd) *(f32x4*)(dst + wh * 1024 + (qd >> 1) * 512 + l * 8 + (qd & 1) * 4) = kv[qd & 1][qd >> 1];
      if (c == 0) { *(f32x4*)(dst + 2048 + wh * 32 + g * 8) = zz[0]; *(f32x4*)(dst + 2048 + wh * 32 + g * 8 + 4) = zz[1]; }
    }
    ST(5);
    lds_barrier();
    ST(6);
    const f32x4 cq0 = *(const f32x4*)(s_cs + wh * 32 + 4 * g), cq1 = *(const f32x4*)(s_cs + wh * 32 + 16 + 4 * g);
    const unsigned pqo = WS_PQ + (unsigned)((panel * 32 + 2 * ct + wh) * 8 + 4 * wr) * 1024u + l * 16;
#pragma unroll
    for (int i = 0; i < 4; ++i) {
      const float rs = s_rs[64 * wr + 16 * i + c], nm = s_rs[128 + 64 * wr + 16 * i + c];
      st16<AUX_PQ>(rw, pqo + i * 1024, __builtin_bit_cast(u32x4, pack8(phi4(acc[0][i] * rs + (nm * cq0 + 1e-6f * LOG2E)),
                                                                      phi4(acc[1][i] * rs + (nm * cq1 + 1e-6f * LOG2E)))));
    }
    ST(7);
  }
  ST(8);
  ST_END(xoff == WS_X1 ? 2 : 4, ws + WS_DBG);
}

#define KO_ZSLP 0
#define KO_SLP 1
#define KO_SLP0 4
constexpr int KO_STAGE = 28672;
constexpr int KO_X = 4 * KO_STAGE;
constexpr int KO_Z = KO_X + 32768;
#define KO_XP 0
constexpr int KO_LDS = KO_Z + 2048 + 256;

template <bool LAST>
__global__ __launch_bounds__(512, 2) void k_out(char* __restrict__ ws, unsigned xoff, char* __restrict__ out) {
  extern __shared__ __attribute__((aligned(16))) char smem[];
  ST_DECL
  const int b = blockIdx.x, w = __builtin_amdgcn_readfirstlane(threadIdx.x >> 6), l = threadIdx.x & 63;
  const int xcd = b & 7, slot = b >> 3;
  const int p64 = 8 * (xcd & 3) + (slot & 7), ct = 4 * (xcd >> 2) + (slot >> 3);
  const rsrc_t rw = make_rsrc(ws, WS_END);
  if (w >= 4) {
    const int lw = w - 4;
    const unsigned lbase = (unsigned)(size_t)(LAS char*)smem;
    constexpr unsigned kvf = WS_KVF + (LAST ? KVF_BYTES : 0);
    const unsigned vq = WS_PQ + (p64 >> 1) * (32 * 8192) + (lw >> 1) * 8192 + (p64 & 1) * 4096 + (lw & 1) * 2048 + l * 16;
    const unsigned vw = WS_WO + (2 * ct + (lw >> 1)) * (1024 * 128) + (lw & 1) * 4096 + l * 16;
    const unsigned vk = kvf + lw * 1024 + l * 16;
    const unsigned vx = (p64 >> 1) * 262144 + (ct * 32 + 8 * lw + (l >> 5)) * 1024 + (p64 & 1) * 512 + (l & 31) * 16;
    auto stage = [&](int u, int sl) {
      const unsigned sb = lbase + sl * KO_STAGE;
      blds_n<1>(rw, vk, u * 4096, sb + 24576 + lw * 1024);
      blds_n<2, POL_A>(rw, vq, u * 16384, sb + lw * 2048);
      blds_n<4, POL_W>(rw, vw, u * 8192, sb + 8192 + lw * 4096);
    };
    auto xtile = [&](unsigned off, int o) {
#pragma unroll
      for (int i = 0; i < 4; ++i) blds_n<1>(rw, vx, off + i * 2048, lbase + KO_X + o + (4 * lw + i) * 1024);
    };
    if (lw < 2) blds_n<1>(rw, vk, 65536, lbase + KO_Z + lw * 1024);
    stage(0, 0); stage(1, 1);
    ST(2);
    WAIT_VM(7);
    __builtin_amdgcn_s_barrier();
    stage(2, 2);
    for (int u = 0; u < 13; ++u) {
      stage(u + 3, (u + 3) & 3);
      WAIT_VM(14);
      __builtin_amdgcn_s_barrier();
    }
    xtile(xoff, 0);
    if (!LAST && KO_XP) { xtile(WS_Y, 16384); WAIT_VM(15); } else { WAIT_VM(11); }
    __builtin_amdgcn_s_barrier();
    if (!LAST && !KO_XP) xtile(WS_Y, 16384);
    WAIT_VM(LAST ? 4 : 8);
    __builtin_amdgcn_s_barrier();
    WAIT_VM(LAST ? 0 : 4);
    __builtin_amdgcn_s_barrier();
    __builtin_amdgcn_s_barrier();
    if (!LAST) {
      WAIT_VM(0);
      __builtin_amdgcn_s_barrier();
    }
    if (!LAST) {
      const int t = threadIdx.x - 256;
      __builtin_amdgcn_s_sleep(KO_ZSLP);
#pragma unroll
      for (int i = 0; i < 3; ++i) st16<16>(rw, t + 256 * i < 528 ? WS_KVP + (b * 528 + t + 256 * i) * 16 : 0xfffffff0u, (u32x4){0, 0, 0, 0});
    }
  } else {
    const int rh = w & 1, hp = w >> 1, c = l & 15, g = l >> 4;
    const int row = 32 * rh + 16 * hp + c;
    const unsigned go = (p64 >> 1) * 262144 + (ct * 32 + g) * 1024 + (p64 & 1) * 512 + ((row ^ ((g & 1) * 16)) * 8);
    f32x4 acc[2][8];
#pragma unroll
    for (int s = 0; s < 2; ++s)
#pragma unroll
      for (int n = 0; n < 8; ++n) { acc[s][n] = (f32x4){0.f, 0.f, 0.f, 0.f}; asm volatile("" : "+v"(acc[s][n])); }
    u32x2 sel = {0u, 0u};
    if (g == (c >> 2)) sel[(c >> 1) & 1] = (c & 1) ? 0x3f800000u : 0x3f80u;
    u32x4 sel0 = {sel[0], sel[1], 0u, 0u}, sel1 = {0u, 0u, sel[0], sel[1]};
    const lchar* px = (lchar*)smem + KO_X + g * 512 + ((row ^ ((g & 1) * 16)) * 8);
    asm volatile("" : "+v"(sel0), "+v"(sel1), "+v"(px));
    bf16x8 fq[2], fkv[2], fz, fw[2][8], fo[2];
    f32x4 num[2][2], den[2];
    const f32x4 zero4 = {0.f, 0.f, 0.f, 0.f};
    f32x4 eps4 = {1e-6f, 1e-6f, 1e-6f, 1e-6f};
    asm volatile("" : "+v"(eps4));
    auto M1 = [&](int s) {
      den[s] = MFMA16(fz, fq[s], eps4);
      num[s][0] = MFMA16(fkv[0], fq[s], zero4);
      num[s][1] = MFMA16(fkv[1], fq[s], zero4);
    };
    auto V = [&](int s) {
      const float inv = __builtin_amdgcn_rcpf(den[s][0]);
      f32x4 o0, o1;
#pragma unroll
      for (int e = 0; e < 4; ++e) { o0[e] = mul_nopk(num[s][0][e], inv); o1[e] = mul_nopk(num[s][1][e], inv); }
      fo[s] = pack8(o0, o1);
    };
    auto M2 = [&](int bf, int s) {
#pragma unroll
      for (int n = 0; n < 8; ++n) acc[s][n] = MFMA16(fw[bf][n], fo[s], acc[s][n]);
    };
    const lchar* pq = (lchar*)smem + hp * 4096 + rh * 2048 + hp * 1024 + l * 16;
    const lchar* pq2 = pq + (hp ? -1024 : 1024);
    const lchar* pw = (lchar*)smem + 8192 + hp * 4096 + l * 16;
    const lchar* pk = (lchar*)smem + 24576 + hp * 2048 + l * 16;
    const lchar* pz = (lchar*)smem + KO_Z + hp * 64 + g * 16;
    int sl = 0;
    auto adv = [&]() {
      const int d = sl == 3 ? -3 * KO_STAGE : KO_STAGE;
      sl = (sl + 1) & 3;
      pq += d; pq2 += d; pk += d; pz += 128; pw += d;
      asm volatile("" : "+v"(pq), "+v"(pq2), "+v"(pw), "+v"(pk), "+v"(pz));
    };
    auto rd = [&](int bf) {
      fz = lds_b128(pz); fq[0] = lds_b128(pq); fkv[0] = lds_b128(pk); fkv[1] = lds_b128(pk + 1024); fq[1] = lds_b128(pq2);
#pragma unroll
      for (int n = 0; n < 8; ++n) fw[bf][n] = lds_b128(pw + (n >> 2) * 8192 + (n & 3) * 1024);
    };
    auto period = [&](int bf) {
      rd(bf ^ 1);
      M2(bf, 0); V(1);
#pragma unroll
      for (int i = 0; i < 5; ++i) { SGB(0x8, 1); SGB(0x100, 2); SGB(0x2, 1); }
#pragma unroll
      for (int i = 0; i < 3; ++i) { SGB(0x8, 1); SGB(0x100, 1); SGB(0x2, 3); }
      SB0();
      asm volatile("" :: "v"(den[1]));
      M1(0); M1(1); adv();
      SB0();
      M2(bf, 1);
      V(0);
#pragma unroll
      for (int i = 0; i < 8; ++i) { SGB(0x8, 1); SGB(0x2, 2); }
      SB0();
      asm volatile("" :: "v"(den[0]));
    };
    ST(2);
    lds_barrier();
    ST(3);
    rd(0);
    adv();
    M1(0); M1(1); V(0);
    SB0();
    for (int u = 0; u < 14; u += 2) {
      lds_barrier();
      SB0();
      period(0);
      lds_barrier();
      SB0();
      period(1);
    }
    lds_barrier();
    SB0();
    period(0);
    lds_barrier();
    SB0();
    bf16x8 fx[4];
#pragma unroll
    for (int k = 0; k < 4; ++k) fx[k] = lds_2b64(px + k * 4096, px + k * 4096 + 2048);
    V(1);
    M2(1, 1);
    SB0();
    {
      char* xb = smem + 16384 + w * 8192 + l * 16;
      M2(1, 0);
#pragma unroll
      for (int n = 0; n < 8; ++n) {
        acc[0][n] = MFMA16(__builtin_bit_cast(bf16x8, (n & 1) ? sel1 : sel0), fx[n >> 1], acc[0][n]);
        *(f32x4*)(xb + n * 1024) = acc[1][n];
      }
#pragma unroll
      for (int n = 0; n < 8; ++n) { SGB(0x8, 2); SGB(0x200, 1); }
    }
    ST(4);
    lds_barrier();
    ST(5);
#pragma unroll
    for (int n = 0; n < 8; ++n) acc[0][n] += *(const f32x4*)(smem + 16384 + (w ^ 2) * 8192 + l * 16 + n * 1024);
    if (LAST) {
      char* tb = smem + w * 4096;
#pragma unroll
      for (int n = 0; n < 8; ++n) *(u32x2*)(tb + c * 256 + (((2 * n + (g >> 1)) ^ c) * 16) + (g & 1) * 8) = (u32x2){pk_bf(acc[0][n][0], acc[0][n][1]), pk_bf(acc[0][n][2], acc[0][n][3])};
      char* o = out + (size_t)(p64 * 64 + 32 * rh + 16 * hp + g) * 2048 + ct * 256 + c * 16;
#pragma unroll
      for (int k = 0; k < 4; ++k) *(u32x4*)(o + k * 8192) = *(const u32x4*)(tb + (4 * k + g) * 256 + ((c ^ (4 * k + g)) * 16));
    } else {
      f32x4 p1 = zero4, p2 = zero4;
#pragma unroll
      for (int n = 0; n < 8; ++n) {
        p1 += acc[0][n];
        p2 += acc[0][n] * acc[0][n];
      }
      const float s1 = swap32_add(swap16_add((p1[0] + p1[1]) + (p1[2] + p1[3])));
      const float s2 = swap32_add(swap16_add((p2[0] + p2[1]) + (p2[2] + p2[3])));
      const unsigned eo = WS_EX + (p64 * (8 * 64) + row) * 8;
      if (l < 16) st8<16>(rw, eo + ct * 512, (u32x2){__float_as_uint(s1), __float_as_uint(s2) | 1u});
      ST(6);
      lds_barrier();
#pragma unroll
      for (int n = 0; n < 8; ++n) {
        const u32x2 y = *(const LAS u32x2*)(px + 16384 + n * 2048);
        acc[1][n] = unpack4(y);
        asm volatile("" : "+v"(acc[1][n]));
      }
      u32x2 gv[2];
      __builtin_amdgcn_s_sleep(KO_SLP0);
      for (unsigned spin = 0; spin < (1u << 17); ++spin) {
#pragma unroll
        for (int i = 0; i < 2; ++i) gv[i] = __builtin_amdgcn_raw_buffer_load_b64(rw, eo + (2 * g + i) * 512, 0, 16);
        if (__all(gv[0][1] & gv[1][1] & 1)) break;
        __builtin_amdgcn_s_sleep(KO_SLP);
        asm volatile("" ::: "memory");
      }
      ST(7);
      const float mean = swap32_add(swap16_add(__uint_as_float(gv[0][0]) + __uint_as_float(gv[1][0]))) * (1.f / D_);
      const float ex2 = swap32_add(swap16_add(__uint_as_float(gv[0][1]) + __uint_as_float(gv[1][1]))) * (1.f / D_);
      const float rs = __builtin_amdgcn_rsqf(fmaxf(ex2 - mean * mean, 0.f) + 1e-5f), nm = -mean * rs;
#pragma unroll
      for (int n = 0; n < 8; ++n) {
        const f32x4 v = acc[0][n] + (acc[0][n] * rs + nm) * acc[1][n];
        st8<AUX_X3>(rw, WS_X3 + go + n * 4096, (u32x2){pk_bf(v[0], v[1]), pk_bf(v[2], v[3])});
      }
    }
  }
  ST(8);
  ST_END(LAST ? 5 : 3, ws + WS_DBG);
}

extern "C" void kernel_launch(void* const* d_in, const int* in_sizes, int n_in, void* d_out, int out_size, void* d_ws,
                              size_t ws_size, hipStream_t stream) {
  const u16* x0 = (const u16*)d_in[0];
  const u16* kin = (const u16*)d_in[1];
  const u16* vin = (const u16*)d_in[2];
  const u16* sk = (const u16*)d_in[3];
  const u16* hk = (const u16*)d_in[4];
  const u16* dv = (const u16*)d_in[5];
  const u16* wqkv = (const u16*)d_in[7];
  const u16* wo = (const u16*)d_in[8];
  char* ws = (char*)d_ws;
  (void)hipFuncSetAttribute((const void*)k_conv, hipFuncAttributeMaxDynamicSharedMemorySize, CV_LDS);
  (void)hipFuncSetAttribute((const void*)k_qkv, hipFuncAttributeMaxDynamicSharedMemorySize, QK_LDS);
  (void)hipFuncSetAttribute((const void*)k_out<false>, hipFuncAttributeMaxDynamicSharedMemorySize, KO_LDS);
  (void)hipFuncSetAttribute((const void*)k_out<true>, hipFuncAttributeMaxDynamicSharedMemorySize, KO_LDS);
  k_prep<<<256, 512, 0, stream>>>(x0, kin, vin, sk, ws);
  k_conv<<<256, 512, CV_LDS, stream>>>(hk, dv, wqkv, wo, ws);
  k_qkv<<<256, 512, QK_LDS, stream>>>(ws, WS_X1, 0);
  k_out<false><<<256, 512, KO_LDS, stream>>>(ws, WS_X1, (char*)d_out);
  k_qkv<<<256, 512, QK_LDS, stream>>>(ws, WS_X3, 1);
  k_out<true><<<256, 512, KO_LDS, stream>>>(ws, WS_X3, (char*)d_out);
}
```

```cpp
#include <hip/hip_runtime.h>
#include <stdint.h>

typedef unsigned short u16;
typedef __bf16 bf16x2 __attribute__((ext_vector_type(2)));
typedef __bf16 bf16x4 __attribute__((ext_vector_type(4)));
typedef __bf16 bf16x8 __attribute__((ext_vector_type(8)));
typedef float f32x2 __attribute__((ext_vector_type(2)));
typedef float f32x4 __attribute__((ext_vector_type(4)));
typedef unsigned u32x2 __attribute__((ext_vector_type(2)));
typedef unsigned u32x4 __attribute__((ext_vector_type(4)));
typedef __amdgpu_buffer_rsrc_t rsrc_t;
#define LAS __attribute__((address_space(3)))
#define DI __device__ __forceinline__
typedef LAS char lchar;

#define ST_DECL
#define ST(i)
#define ST_END(k, dbg)
constexpr size_t WS_DBG = 64u << 20;

constexpr int S_ = 2048, D_ = 1024;
constexpr unsigned WS_UT = 0;
constexpr unsigned WS_X1 = 4u << 20;
constexpr unsigned WS_Y = 8u << 20;
constexpr unsigned WS_X3 = 12u << 20;
constexpr unsigned WS_PQ = 16u << 20;
constexpr unsigned WS_WQ = 20u << 20;
constexpr unsigned WS_WO = 26u << 20;
constexpr unsigned WS_XQ = 28u << 20;
constexpr unsigned WS_ST0 = 32u << 20;
constexpr unsigned WS_CS = WS_ST0 + 65536;
constexpr unsigned WS_KVF = WS_CS + 196608;
constexpr unsigned KVF_BYTES = 65536 + 2048;
constexpr unsigned WS_ZERO = WS_KVF + 2 * KVF_BYTES;
constexpr unsigned WS_EX = WS_ZERO;
constexpr unsigned WS_RS = WS_EX + 32 * 8 * 64 * 8;
constexpr unsigned ZERO_BYTES = WS_RS + 2 * 262144 - WS_ZERO;
static_assert(ZERO_BYTES == 256 * 160 * 16, "zeroed region");
constexpr unsigned WS_KVP = 34u << 20;
constexpr unsigned KVP_BLK = 2112 * 4;
constexpr unsigned WS_NONCE = 38u << 20;
constexpr unsigned WS_FU = WS_NONCE + 4096;
constexpr unsigned WS_FX = WS_FU + 65536;
constexpr unsigned WS_END = 40u << 20;
constexpr int NT = 2;

#define AUX_UT 16
#define AUX_XQ 0
#define AUX_X1 16
#define AUX_Z 16
#define AUX_W 16
#define AUX_PQ 16
#define AUX_X3 16

extern "C" __device__ float mul_nopk(float, float) __asm("llvm.amdgcn.fmul.legacy");
DI float bf_lo(unsigned u) { return __uint_as_float(u << 16); }
DI float bf_hi(unsigned u) { return __uint_as_float(u & 0xffff0000u); }
DI unsigned pk_bf(float lo, float hi) {
  f32x2 v = {lo, hi};
  return __builtin_bit_cast(unsigned, __builtin_convertvector(v, bf16x2));
}
template <int CTRL> DI float dpp_mov(float x) {
  return __int_as_float(__builtin_amdgcn_update_dpp(0, __float_as_int(x), CTRL, 0xf, 0xf, true));
}
DI float swap32_add(float x) {
  auto r = __builtin_amdgcn_permlane32_swap(__float_as_uint(x), __float_as_uint(x), false, false);
  return __uint_as_float(r[0]) + __uint_as_float(r[1]);
}
DI float swap16_add(float x) {
  auto r = __builtin_amdgcn_permlane16_swap(__float_as_uint(x), __float_as_uint(x), false, false);
  return __uint_as_float(r[0]) + __uint_as_float(r[1]);
}
DI float wave_sum(float x) {
  x += dpp_mov<0xB1>(x);
  x += dpp_mov<0x4E>(x);
  x += dpp_mov<0x141>(x);
  x += dpp_mov<0x140>(x);
  return swap32_add(swap16_add(x));
}
DI void lds_barrier() {
  asm volatile("" ::: "memory");
  __builtin_amdgcn_s_waitcnt(0xC07F);
  __builtin_amdgcn_s_barrier();
  asm volatile("" ::: "memory");
}
#define SB0() __builtin_amdgcn_sched_barrier(0)
#define SGB(mask, n) __builtin_amdgcn_sched_group_barrier(mask, n, 0)
#define WAIT_VM(n) asm volatile("s_waitcnt vmcnt(%0)" :: "n"(n) : "memory")
#define MFMA16(a, b, c) __builtin_amdgcn_mfma_f32_16x16x32_bf16((a), (b), (c), 0, 0, 0)
DI bf16x8 lds_b128(const char* p) { return *(const bf16x8*)p; }
DI bf16x8 lds_b128(const lchar* p) { return *(const LAS bf16x8*)p; }
DI bf16x8 lds_tr2(const char* p0, const char* p1) {
  bf16x4 a = __builtin_amdgcn_ds_read_tr16_b64_v4bf16((LAS bf16x4*)p0);
  bf16x4 b = __builtin_amdgcn_ds_read_tr16_b64_v4bf16((LAS bf16x4*)p1);
  return __builtin_shufflevector(a, b, 0, 1, 2, 3, 4, 5, 6, 7);
}
DI bf16x8 lds_2b64(const lchar* p0, const lchar* p1) {
  const u32x2 a = *(const volatile LAS u32x2*)p0, b = *(const volatile LAS u32x2*)p1;
  const u32x4 r = {a[0], a[1], b[0], b[1]};
  return __builtin_bit_cast(bf16x8, r);
}
DI f32x4 unpack4(u32x2 v) { return (f32x4){bf_lo(v[0]), bf_hi(v[0]), bf_lo(v[1]), bf_hi(v[1])}; }
DI bf16x8 pack8(f32x4 a, f32x4 b) {
  u32x4 p = {pk_bf(a[0], a[1]), pk_bf(a[2], a[3]), pk_bf(b[0], b[1]), pk_bf(b[2], b[3])};
  return __builtin_bit_cast(bf16x8, p);
}
DI rsrc_t make_rsrc(const void* p, unsigned bytes) { return __builtin_amdgcn_make_buffer_rsrc((void*)p, (short)0, (int)bytes, 0x00020000); }
#define BLDS_(o, p) "buffer_load_dwordx4 %1, %2, %3 offen offset:" #o p " lds\n\t"
#define BLDS_ASM(str) asm volatile("s_mov_b32 m0, %0\n\ts_nop 0\n\t" str :: "s"(lds_base), "v"(voff), "s"(rsrc), "s"(soff) : "memory")
#define BLDS_ALL(p)                                                                       \
  if constexpr (N == 1) BLDS_ASM(BLDS_(0, p));                                            \
  else if constexpr (N == 2) BLDS_ASM(BLDS_(0, p) BLDS_(1024, p));                        \
  else if constexpr (N == 3) BLDS_ASM(BLDS_(0, p) BLDS_(1024, p) BLDS_(2048, p));         \
  else BLDS_ASM(BLDS_(0, p) BLDS_(1024, p) BLDS_(2048, p) BLDS_(3072, p));
template <int N, int P = 0> DI void blds_n(rsrc_t rsrc, unsigned voff, unsigned soff, unsigned lds_base) {
  if constexpr (P == 0) { BLDS_ALL("") } else if constexpr (P == 1) { BLDS_ALL(" nt") } else { BLDS_ALL(" sc1") }
}
#define POL_A 0
#define POL_W 0
#define CODE_WARM(sym, bytes, t)                                                                                        \
  do {                                                                                                                  \
    unsigned lo_, hi_;                                                                                                  \
    asm volatile("s_getpc_b64 s[40:41]\n\ts_add_u32 %0, s40, " sym "@rel32@lo+4\n\ts_addc_u32 %1, s41, " sym "@rel32@hi+12" \
                 : "=&s"(lo_), "=&s"(hi_) :: "scc", "s40", "s41");                                                      \
    if ((t) * 128 < (bytes)) {                                                                                          \
      const unsigned v_ = *(const volatile __attribute__((address_space(1))) unsigned*)((((size_t)hi_ << 32) | lo_) + (t) * 128); \
      asm volatile("" :: "v"(v_));                                                                                      \
    }                                                                                                                   \
  } while (0)
DI unsigned opaque(unsigned v) { asm volatile("" : "+v"(v) :: "memory"); return v; }
template <int AUX> DI void st8(rsrc_t r, unsigned voff, u32x2 v) { __builtin_amdgcn_raw_buffer_store_b64(v, r, voff, 0, AUX); }
template <int AUX> DI void st16(rsrc_t r, unsigned voff, u32x4 v) { __builtin_amdgcn_raw_buffer_store_b128(v, r, voff, 0, AUX); }

#define QK_D 2
#define QK_EXP 0
#define QK_SLP0 8
constexpr int QK_STAGE = 40960, QK_NSL = 3;
constexpr int QK_RS = QK_NSL * QK_STAGE;
constexpr int QK_KV = QK_RS + 2048;
constexpr int QK_LDS = QK_KV + 24576;
#define QK_P 4
constexpr float LOG2E = 1.44269504088896f, LN2 = 0.693147180559945f;

DI f32x4 phi4(f32x4 u) {
  f32x4 e, m;
#pragma unroll
  for (int i = 0; i < 4; ++i) {
    e[i] = __builtin_amdgcn_fmed3f(__builtin_amdgcn_exp2f(u[i]), 0.f, 1.f);
    m[i] = fmaxf(u[i], 1e-6f * LOG2E);
  }
  return m * LN2 + e;
}

extern "C" __global__ __launch_bounds__(512, 2) void k_qkv(char* ws, unsigned xoff, unsigned layer) {
  extern __shared__ __attribute__((aligned(16))) char smem[];
  ST_DECL
  const int b = blockIdx.x, w = __builtin_amdgcn_readfirstlane(threadIdx.x >> 6), l = threadIdx.x & 63;
  const int xcd = b & 7, slot = b >> 3;
  const int panel = 8 * (xcd & 1) + (slot & 7), ct = 4 * (xcd >> 1) + (slot >> 3);
  const rsrc_t rw = make_rsrc(ws, WS_END);
  float* s_rs = (float*)(smem + QK_RS);
  float* s_cs = s_rs + 256;
  float* s_kv = (float*)(smem + QK_KV);
  const unsigned rsoff = WS_RS + layer * 262144 + panel * 16384;
  const int lw = w & 3;
  const unsigned lbase = (unsigned)(size_t)(LAS char*)smem;
  const unsigned va = xoff + panel * 262144 + lw * 2048 + l * 16;
  const unsigned vb = WS_WQ + ct * (32 * 12288) + lw * 3072 + l * 16;
  auto issue = [&](int t, int sl, int h) {
    const int o = t < QK_P ? t : t - 1;
    const int ks = 2 * (t == QK_P ? ct : (o < ct ? o : o + 1)) + h;
    const unsigned sb = lbase + sl * QK_STAGE;
    blds_n<2>(rw, va, ks * 8192, sb + (4 * h + lw) * 2048);
    blds_n<3>(rw, vb, ks * 12288, sb + 16384 + h * 12288 + lw * 3072);
  };
  issue(0, 0, w >> 2);
  if (w >= 4) {
#define LBAR() __builtin_amdgcn_s_barrier()
    issue(1, 1, 0);
    const int t = threadIdx.x - 256, c = l & 15, g = l >> 4;
    unsigned ro = rsoff + 4 * g * 1024 + (32 * lw + 2 * c) * 8;
    unsigned co = t < 192 ? WS_CS + (4 * g * 3072 + lw * 1024 + ct * 64 + c * 4) * 4 : 0xfffffff0u;
    LAS float* wrs = (LAS float*)s_rs + (g >> 1) * 128 + 32 * lw + 2 * c + (g & 1);
    LAS float* wcs = (LAS float*)s_cs + lw * 64 + c * 4 + g;
    asm volatile("" : "+v"(ro), "+v"(co), "+v"(wrs), "+v"(wcs));
    ST(2);
    WAIT_VM(5);
    LBAR();
    issue(1, 1, 1);
    int sl = 2;
    for (int u = 0; u < 13; ++u) {
      issue(u + 2, sl, 0); issue(u + 2, sl, 1);
      sl = sl == 2 ? 0 : sl + 1;
      WAIT_VM(10);
      LBAR();
    }
    issue(15, 0, 0); issue(15, 0, 1);
    u32x4 xr[4];
#pragma unroll
    for (int i = 0; i < 4; ++i) xr[i] = __builtin_amdgcn_raw_buffer_load_b128(rw, ro, i * 1024, 16);
    u32x4 cr[4];
#pragma unroll
    for (int i = 0; i < 4; ++i) cr[i] = __builtin_amdgcn_raw_buffer_load_b128(rw, co, i * 12288, 0);
    SB0();
    WAIT_VM(18);
    LBAR();
    SB0();
    __builtin_amdgcn_s_setprio(2);
#pragma unroll 1
    for (unsigned spin = 0; spin < (1u << 17); ++spin) {
      if (__all(xr[0][1] & xr[0][3] & xr[1][1] & xr[1][3] & xr[2][1] & xr[2][3] & xr[3][1] & xr[3][3] & 1)) break;
      __builtin_amdgcn_s_sleep(1);
      ro = opaque(ro);
#pragma unroll
      for (int i = 0; i < 4; ++i) xr[i] = __builtin_amdgcn_raw_buffer_load_b128(rw, ro, i * 1024, 16);
      asm volatile("" ::: "memory");
    }
    __builtin_amdgcn_s_waitcnt(0x0F70);
    SB0();
    __builtin_amdgcn_s_barrier();
    SB0();
    {
      float p[4], c0[4];
#pragma unroll
      for (int e = 0; e < 4; ++e) {
        float a = __uint_as_float(xr[0][e]) + __uint_as_float(xr[1][e]), d = __uint_as_float(xr[2][e]) + __uint_as_float(xr[3][e]);
        asm("" : "+v"(a));
        p[e] = a + d;
        asm("" : "+v"(p[e]));
        a = __uint_as_float(cr[0][e]) + __uint_as_float(cr[1][e]); d = __uint_as_float(cr[2][e]) + __uint_as_float(cr[3][e]);
        asm("" : "+v"(a));
        c0[e] = a + d;
        asm("" : "+v"(c0[e]));
      }
      auto r0 = __builtin_amdgcn_permlane16_swap(__float_as_uint(p[0]), __float_as_uint(p[2]), false, false);
      auto r1 = __builtin_amdgcn_permlane16_swap(__float_as_uint(p[1]), __float_as_uint(p[3]), false, false);
      const float mean = swap32_add(__uint_as_float(r0[0]) + __uint_as_float(r0[1])) * (1.f / D_);
      const float ex2 = swap32_add(__uint_as_float(r1[0]) + __uint_as_float(r1[1])) * (1.f / D_);
      const float rs = __builtin_amdgcn_rsqf(fmaxf(ex2 - mean * mean, 0.f) + 1e-5f) * LOG2E;
      *wrs = (g >> 1) ? -mean * rs : rs;
      auto q0 = __builtin_amdgcn_permlane16_swap(__float_as_uint(c0[0]), __float_as_uint(c0[1]), false, false);
      auto q1 = __builtin_amdgcn_permlane16_swap(__float_as_uint(c0[2]), __float_as_uint(c0[3]), false, false);
      auto q2 = __builtin_amdgcn_permlane32_swap(__float_as_uint(__uint_as_float(q0[0]) + __uint_as_float(q0[1])),
                                                 __float_as_uint(__uint_as_float(q1[0]) + __uint_as_float(q1[1])), false, false);
      *wcs = __uint_as_float(q2[0]) + __uint_as_float(q2[1]);
    }
    ST(3);
    lds_barrier();
    if (layer == 0 && slot == 0 && lw >= 2) CODE_WARM("k_out", 7168, t - 128);
    ST(4);
    lds_barrier();
    ST(5);
    const unsigned pb = WS_KVP + ct * (16 * KVP_BLK);
#pragma unroll
    for (int i = 0; i < 3; ++i) {
      const int e = t + 256 * i;
      u32x4 v = __builtin_bit_cast(u32x4, *(const f32x4*)(s_kv + e * 4) + *(const f32x4*)(s_kv + 2112 + e * 4));
      v[1] |= 1u; v[3] |= 1u;
      st16<16>(rw, e < 528 ? pb + panel * KVP_BLK + e * 16 : 0xfffffff0u, v);
    }
    ST(6);
    char* kvf = ws + WS_KVF + layer * KVF_BYTES;
    __builtin_amdgcn_s_sleep(QK_SLP0);
    if (lw == 0) {
      unsigned go = pb + (8 * (l >> 5)) * KVP_BLK + panel * 512 + (l & 31) * 16;
      u32x4 v[8];
#pragma unroll 1
      for (unsigned spin = 0; spin < (1u << 17); ++spin) {
        unsigned ok = 1;
        go = opaque(go);
#pragma unroll
        for (int i = 0; i < 8; ++i) {
          v[i] = __builtin_amdgcn_raw_buffer_load_b128(rw, go, i * KVP_BLK, 16);
          ok &= v[i][1] & v[i][3];
        }
        if (__all(ok)) break;
        asm volatile("" ::: "memory");
      }
      f32x4 a = __builtin_bit_cast(f32x4, v[0]);
#pragma unroll
      for (int i = 1; i < 8; ++i) a += __builtin_bit_cast(f32x4, v[i]);
#pragma unroll
      for (int e = 0; e < 4; ++e) a[e] = swap32_add(a[e]);
      if (l < 32) *(u32x2*)(kvf + ct * 4096 + panel * 256 + l * 8) = (u32x2){pk_bf(a[0], a[1]), pk_bf(a[2], a[3])};
    } else if (lw == 1 && panel < 8) {
      const unsigned go = pb + ((l >> 1) & 15) * KVP_BLK + 8192 + panel * 32 + (l & 1) * 16;
      u32x4 v;
#pragma unroll 1
      for (unsigned spin = 0; spin < (1u << 17); ++spin) {
        v = __builtin_amdgcn_raw_buffer_load_b128(rw, opaque(go), 0, 16);
        if (__all(v[1] & v[3] & 1)) break;
        asm volatile("" ::: "memory");
      }
      f32x4 a = __builtin_bit_cast(f32x4, v);
#pragma unroll
      for (int e = 0; e < 4; ++e) {
        a[e] += dpp_mov<0x4E>(a[e]);
        a[e] += dpp_mov<0x124>(a[e]);
        a[e] += dpp_mov<0x128>(a[e]);
        a[e] = swap16_add(a[e]);
      }
      if (l < 2) *(u32x2*)(kvf + 65536 + ct * 128 + panel * 16 + l * 8) = (u32x2){pk_bf(a[0], a[1]), pk_bf(a[2], a[3])};
    }
    ST(7);
  } else {
    const int wr = w >> 1, wh = w & 1, c = l & 15, g = l >> 4;
    f32x4 acc[6][4];
#pragma unroll
    for (int j = 0; j < 6; ++j)
#pragma unroll
      for (int i = 0; i < 4; ++i) { acc[j][i] = (f32x4){0.f, 0.f, 0.f, 0.f}; asm volatile("" : "+v"(acc[j][i])); }
    const lchar* qa = (lchar*)smem + g * 1024 + (64 * wr + c) * 8 + (g & 1) * 128;
    const lchar* qa2 = qa + ((g & 1) ? -128 : 128);
    const lchar* qb = (lchar*)smem + 16384 + wh * 2048 + l * 16;
    bf16x8 fa[2][4], fb[2][6];
    auto ldf = [&](int buf, int h) {
      fa[buf][0] = lds_2b64(qa + h * 8192, qa + h * 8192 + 4096);
      fb[buf][0] = lds_b128(qb + h * 12288);
      fa[buf][1] = lds_2b64(qa2 + h * 8192, qa2 + h * 8192 + 4096);
      fa[buf][2] = lds_2b64(qa + h * 8192 + 256, qa + h * 8192 + 256 + 4096);
      fa[buf][3] = lds_2b64(qa2 + h * 8192 + 256, qa2 + h * 8192 + 256 + 4096);
#pragma unroll
      for (int j = 1; j < 6; ++j) fb[buf][j] = lds_b128(qb + h * 12288 + (j >> 1) * 4096 + (j & 1) * 1024);
    };
    auto mma = [&](int buf, int m0, int m1) {
#pragma unroll
      for (int m = m0; m < m1; ++m) {
        const int j = m >> 2, i = m & 3;
        acc[j][i] = j < 2 ? MFMA16(fb[buf][j], fa[buf][i], acc[j][i])
                          : MFMA16(fa[buf][i], fb[buf][j], acc[j][i]);
      }
    };
    u32x4 one4 = {0x3f803f80u, 0x3f803f80u, 0x3f803f80u, 0x3f803f80u};
    const f32x4 zero4 = {0.f, 0.f, 0.f, 0.f};
    unsigned po = (c >> 2) == g ? rsoff + ct * 1024 + (64 * wr + 32 * wh + c) * 8 : 0xfffffff0u;
    asm volatile("" : "+v"(qa), "+v"(qa2), "+v"(qb), "+v"(one4), "+v"(po));
    int sl = 0;
    auto tile = [&](bool stats) {
      ldf(1, 1);
      mma(0, 0, 16);
#pragma unroll
      for (int i = 0; i < 14; ++i) { SGB(0x8, 1); SGB(0x100, 1); }
      SGB(0x8, 2);
      SB0();
      const int d = sl == 2 ? -2 * QK_STAGE : QK_STAGE;
      sl = sl == 2 ? 0 : sl + 1;
      qa += d; qa2 += d; qb += d;
      asm volatile("" : "+v"(qa), "+v"(qa2), "+v"(qb));
      mma(0, 16, 24);
      SB0();
      f32x4 m1[2], m2[2];
      if (stats) {
        if (wh) {
#pragma unroll
          for (int i = 0; i < 2; ++i) {
            m1[i] = MFMA16(__builtin_bit_cast(bf16x8, one4), fa[1][2 + i], MFMA16(__builtin_bit_cast(bf16x8, one4), fa[0][2 + i], zero4));
            m2[i] = MFMA16(fa[1][2 + i], fa[1][2 + i], MFMA16(fa[0][2 + i], fa[0][2 + i], zero4));
          }
        } else {
#pragma unroll
          for (int i = 0; i < 2; ++i) {
            m1[i] = MFMA16(__builtin_bit_cast(bf16x8, one4), fa[1][i], MFMA16(__builtin_bit_cast(bf16x8, one4), fa[0][i], zero4));
            m2[i] = MFMA16(fa[1][i], fa[1][i], MFMA16(fa[0][i], fa[0][i], zero4));
          }
        }
        SB0();
      }
      mma(1, 0, 1);
      SB0();
      lds_barrier();
      SB0();
      ldf(0, 0);
      mma(1, 1, 24);
      if (stats) {
#pragma unroll
        for (int i = 0; i < 2; ++i) {
          float e0 = m2[i][0], e1 = m2[i][1], e2 = m2[i][2], e3 = m2[i][3];
          asm("" : "+v"(e0), "+v"(e1), "+v"(e2), "+v"(e3));
          const float a1 = m1[i][0], a2 = (l & 2) ? ((l & 1) ? e3 : e2) : ((l & 1) ? e1 : e0);
          __builtin_amdgcn_raw_buffer_store_b64((u32x2){__float_as_uint(a1), __float_as_uint(a2) | 1u}, rw, po, i * 128, 16);
        }
#pragma unroll
        for (int i = 0; i < 14; ++i) { SGB(0x8, 1); SGB(0x100, 1); SGB(0x2, 1); }
#pragma unroll
        for (int i = 0; i < 2; ++i) { SGB(0x8, 1); SGB(0x2, 2); }
        SGB(0x8, 2); SGB(0x40, 1); SGB(0x8, 2); SGB(0x40, 1); SGB(0x8, 3);
      } else {
#pragma unroll
        for (int i = 0; i < 14; ++i) { SGB(0x8, 1); SGB(0x100, 1); }
        SGB(0x8, 9);
      }
      SB0();
    };
    ST(2);
    WAIT_VM(0);
    lds_barrier();
    ST(3);
    ldf(0, 0);
    SB0();
#pragma unroll 1
    for (int t = 0; t < QK_P; ++t) tile(false);
    tile(true);
#pragma unroll 1
    for (int t = QK_P + 1; t < 15; ++t) tile(false);
    ldf(1, 1);
    mma(0, 0, 24);
#pragma unroll
    for (int i = 0; i < 14; ++i) { SGB(0x8, 1); SGB(0x100, 1); }
    SGB(0x8, 10);
    SB0();
    mma(1, 8, 20);
    SB0();
    lds_barrier();
    SB0();
    mma(1, 20, 24);
    mma(1, 0, 8);
    ST(4);
    {
      f32x4 kv[2][2], zz[2];
      float csk[2], csv[2];
#pragma unroll
      for (int a = 0; a < 2; ++a) {
        kv[a][0] = kv[a][1] = zz[a] = (f32x4){0.f, 0.f, 0.f, 0.f};
        csk[a] = s_cs[64 + wh * 32 + 16 * a + c];
        csv[a] = s_cs[128 + wh * 32 + 16 * a + c];
      }
#pragma unroll
      for (int ip = 0; ip < 2; ++ip) {
        f32x4 rs[2], nm[2];
#pragma unroll
        for (int h = 0; h < 2; ++h) {
          rs[h] = *(const f32x4*)(s_rs + 64 * wr + 32 * ip + 16 * h + 4 * g);
          nm[h] = *(const f32x4*)(s_rs + 128 + 64 * wr + 32 * ip + 16 * h + 4 * g);
        }
        bf16x8 fk[2], fv[2];
#pragma unroll
        for (int a = 0; a < 2; ++a) {
          fk[a] = pack8(phi4(acc[2 + a][2 * ip] * rs[0] + (nm[0] * csk[a] + 1e-6f * LOG2E)),
                        phi4(acc[2 + a][2 * ip + 1] * rs[1] + (nm[1] * csk[a] + 1e-6f * LOG2E)));
        }
#pragma unroll
        for (int h = 0; h < 2; ++h) { rs[h] *= LN2; nm[h] *= LN2; }
#pragma unroll
        for (int a = 0; a < 2; ++a)
          fv[a] = pack8(acc[4 + a][2 * ip] * rs[0] + nm[0] * csv[a], acc[4 + a][2 * ip + 1] * rs[1] + nm[1] * csv[a]);
#pragma unroll
        for (int a = 0; a < 2; ++a) {
          kv[a][0] = MFMA16(fk[a], fv[0], kv[a][0]);
          kv[a][1] = MFMA16(fk[a], fv[1], kv[a][1]);
          zz[a] = MFMA16(fk[a], __builtin_bit_cast(bf16x8, one4), zz[a]);
        }
      }
      float* dst = s_kv + wr * 2112;
#pragma unroll
      for (int qd = 0; qd < 4; ++qd) *(f32x4*)(dst + wh * 1024 + (qd >> 1) * 512 + l * 8 + (qd & 1) * 4) = kv[qd & 1][qd >> 1];
      if (c == 0) { *(f32x4*)(dst + 2048 + wh * 32 + g * 8) = zz[0]; *(f32x4*)(dst + 2048 + wh * 32 + g * 8 + 4) = zz[1]; }
    }
    ST(5);
    lds_barrier();
    ST(6);
    const f32x4 cq0 = *(const f32x4*)(s_cs + wh * 32 + 4 * g), cq1 = *(const f32x4*)(s_cs + wh * 32 + 16 + 4 * g);
    const unsigned pqo = WS_PQ + (unsigned)((panel * 32 + 2 * ct + wh) * 8 + 4 * wr) * 1024u + l * 16;
#pragma unroll
    for (int i = 0; i < 4; ++i) {
      const float rs = s_rs[64 * wr + 16 * i + c], nm = s_rs[128 + 64 * wr + 16 * i + c];
      st16<AUX_PQ>(rw, pqo + i * 1024, __builtin_bit_cast(u32x4, pack8(phi4(acc[0][i] * rs + (nm * cq0 + 1e-6f * LOG2E)),
                                                                      phi4(acc[1][i] * rs + (nm * cq1 + 1e-6f * LOG2E)))));
    }
    ST(7);
  }
  ST(8);
  ST_END(xoff == WS_X1 ? 2 : 4, ws + WS_DBG);
}

#define KO_ZSLP 0
#define KO_SLP 1
#define KO_SLP0 4
constexpr int KO_STAGE = 28672;
constexpr int KO_X = 4 * KO_STAGE;
constexpr int KO_Z = KO_X + 32768;
#define KO_XP 0
constexpr int KO_LDS = KO_Z + 2048 + 256;

extern "C" __global__ __launch_bounds__(512, 2) void k_out(char* ws, char* __restrict__ out, unsigned LAST) {
  extern __shared__ __attribute__((aligned(16))) char smem[];
  ST_DECL
  const int b = blockIdx.x, w = __builtin_amdgcn_readfirstlane(threadIdx.x >> 6), l = threadIdx.x & 63;
  const int xcd = b & 7, slot = b >> 3;
  const int p64 = 8 * (xcd & 3) + (slot & 7), ct = 4 * (xcd >> 2) + (slot >> 3);
  const rsrc_t rw = make_rsrc(ws, WS_END);
  const int lw = w & 3;
  const unsigned lbase = (unsigned)(size_t)(LAS char*)smem;
  const unsigned kvf = WS_KVF + LAST * KVF_BYTES, xoff = LAST ? WS_X3 : WS_X1;
  const unsigned vq = WS_PQ + (p64 >> 1) * (32 * 8192) + (lw >> 1) * 8192 + (p64 & 1) * 4096 + (lw & 1) * 2048 + l * 16;
  const unsigned vw = WS_WO + (2 * ct + (lw >> 1)) * (1024 * 128) + (lw & 1) * 4096 + l * 16;
  const unsigned vk = kvf + lw * 1024 + l * 16;
  const unsigned vx = (p64 >> 1) * 262144 + (ct * 32 + 8 * lw + (l >> 5)) * 1024 + (p64 & 1) * 512 + (l & 31) * 16;
  auto stage = [&](int u, int sl) {
    const unsigned sb = lbase + sl * KO_STAGE;
    blds_n<1>(rw, vk, u * 4096, sb + 24576 + lw * 1024);
    blds_n<2, POL_A>(rw, vq, u * 16384, sb + lw * 2048);
    blds_n<4, POL_W>(rw, vw, u * 8192, sb + 8192 + lw * 4096);
  };
  stage(w >> 2, w >> 2);
  if (w < 2) blds_n<1>(rw, vk, 65536, lbase + KO_Z + lw * 1024);
  if (w >= 4) {
    auto xtile = [&](unsigned off, int o) {
#pragma unroll
      for (int i = 0; i < 4; ++i) blds_n<1>(rw, vx, off + i * 2048, lbase + KO_X + o + (4 * lw + i) * 1024);
    };
    stage(2, 2);
    ST(2);
    __builtin_amdgcn_s_barrier();
    for (int u = 0; u < 13; ++u) {
      stage(u + 3, (u + 3) & 3);
      WAIT_VM(14);
      __builtin_amdgcn_s_barrier();
    }
    xtile(xoff, 0);
    WAIT_VM(11);
    __builtin_amdgcn_s_barrier();
    if (LAST) {
      WAIT_VM(4);
      __builtin_amdgcn_s_barrier();
      WAIT_VM(0);
      __builtin_amdgcn_s_barrier();
      __builtin_amdgcn_s_barrier();
    } else {
      xtile(WS_Y, 16384);
      WAIT_VM(8);
      __builtin_amdgcn_s_barrier();
      WAIT_VM(4);
      __builtin_amdgcn_s_barrier();
      __builtin_amdgcn_s_barrier();
      WAIT_VM(0);
      __builtin_amdgcn_s_barrier();
    }
    if (!LAST) {
      const int t = threadIdx.x - 256;
      __builtin_amdgcn_s_sleep(KO_ZSLP);
#pragma unroll
      for (int i = 0; i < 3; ++i) st16<16>(rw, t + 256 * i < 528 ? WS_KVP + (b * 528 + t + 256 * i) * 16 : 0xfffffff0u, (u32x4){0, 0, 0, 0});
    }
  } else {
    const int rh = w & 1, hp = w >> 1, c = l & 15, g = l >> 4;
    const int row = 32 * rh + 16 * hp + c;
    const unsigned go = (p64 >> 1) * 262144 + (ct * 32 + g) * 1024 + (p64 & 1) * 512 + ((row ^ ((g & 1) * 16)) * 8);
    f32x4 acc[2][8];
#pragma unroll
    for (int s = 0; s < 2; ++s)
#pragma unroll
      for (int n = 0; n < 8; ++n) { acc[s][n] = (f32x4){0.f, 0.f, 0.f, 0.f}; asm volatile("" : "+v"(acc[s][n])); }
    u32x2 sel = {0u, 0u};
    if (g == (c >> 2)) sel[(c >> 1) & 1] = (c & 1) ? 0x3f800000u : 0x3f80u;
    u32x4 sel0 = {sel[0], sel[1], 0u, 0u}, sel1 = {0u, 0u, sel[0], sel[1]};
    const lchar* px = (lchar*)smem + KO_X + g * 512 + ((row ^ ((g & 1) * 16)) * 8);
    asm volatile("" : "+v"(sel0), "+v"(sel1), "+v"(px));
    bf16x8 fq[2], fkv[2], fz, fw[2][8], fo[2];
    f32x4 num[2][2], den[2];
    const f32x4 zero4 = {0.f, 0.f, 0.f, 0.f};
    f32x4 eps4 = {1e-6f, 1e-6f, 1e-6f, 1e-6f};
    asm volatile("" : "+v"(eps4));
    auto M1 = [&](int s) {
      den[s] = MFMA16(fz, fq[s], eps4);
      num[s][0] = MFMA16(fkv[0], fq[s], zero4);
      num[s][1] = MFMA16(fkv[1], fq[s], zero4);
    };
    auto V = [&](int s) {
      const float inv = __builtin_amdgcn_rcpf(den[s][0]);
      f32x4 o0, o1;
#pragma unroll
      for (int e = 0; e < 4; ++e) { o0[e] = mul_nopk(num[s][0][e], inv); o1[e] = mul_nopk(num[s][1][e], inv); }
      fo[s] = pack8(o0, o1);
    };
    auto M2 = [&](int bf, int s) {
#pragma unroll
      for (int n = 0; n < 8; ++n) acc[s][n] = MFMA16(fw[bf][n], fo[s], acc[s][n]);
    };
    const lchar* pq = (lchar*)smem + hp * 4096 + rh * 2048 + hp * 1024 + l * 16;
    const lchar* pq2 = pq + (hp ? -1024 : 1024);
    const lchar* pw = (lchar*)smem + 8192 + hp * 4096 + l * 16;
    const lchar* pk = (lchar*)smem + 24576 + hp * 2048 + l * 16;
    const lchar* pz = (lchar*)smem + KO_Z + hp * 64 + g * 16;
    int sl = 0;
    auto adv = [&]() {
      const int d = sl == 3 ? -3 * KO_STAGE : KO_STAGE;
      sl = (sl + 1) & 3;
      pq += d; pq2 += d; pk += d; pz += 128; pw += d;
      asm volatile("" : "+v"(pq), "+v"(pq2), "+v"(pw), "+v"(pk), "+v"(pz));
    };
    auto rd = [&](int bf) {
      fz = lds_b128(pz); fq[0] = lds_b128(pq); fkv[0] = lds_b128(pk); fkv[1] = lds_b128(pk + 1024); fq[1] = lds_b128(pq2);
#pragma unroll
      for (int n = 0; n < 8; ++n) fw[bf][n] = lds_b128(pw + (n >> 2) * 8192 + (n & 3) * 1024);
    };
    auto period = [&](int bf) {
      rd(bf ^ 1);
      M2(bf, 0); V(1);
#pragma unroll
      for (int i = 0; i < 5; ++i) { SGB(0x8, 1); SGB(0x100, 2); SGB(0x2, 1); }
#pragma unroll
      for (int i = 0; i < 3; ++i) { SGB(0x8, 1); SGB(0x100, 1); SGB(0x2, 3); }
      SB0();
      asm volatile("" :: "v"(den[1]));
      M1(0); M1(1); adv();
      SB0();
      M2(bf, 1);
      V(0);
#pragma unroll
      for (int i = 0; i < 8; ++i) { SGB(0x8, 1); SGB(0x2, 2); }
      SB0();
      asm volatile("" :: "v"(den[0]));
    };
    ST(2);
    WAIT_VM(0);
    lds_barrier();
    ST(3);
    rd(0);
    adv();
    M1(0); M1(1); V(0);
    SB0();
    for (int u = 0; u < 14; u += 2) {
      lds_barrier();
      SB0();
      period(0);
      lds_barrier();
      SB0();
      period(1);
    }
    lds_barrier();
    SB0();
    period(0);
    lds_barrier();
    SB0();
    bf16x8 fx[4];
#pragma unroll
    for (int k = 0; k < 4; ++k) fx[k] = lds_2b64(px + k * 4096, px + k * 4096 + 2048);
    V(1);
    M2(1, 1);
    SB0();
    {
      char* xb = smem + 16384 + w * 8192 + l * 16;
      M2(1, 0);
#pragma unroll
      for (int n = 0; n < 8; ++n) {
        acc[0][n] = MFMA16(__builtin_bit_cast(bf16x8, (n & 1) ? sel1 : sel0), fx[n >> 1], acc[0][n]);
        *(f32x4*)(xb + n * 1024) = acc[1][n];
      }
#pragma unroll
      for (int n = 0; n < 8; ++n) { SGB(0x8, 2); SGB(0x200, 1); }
    }
    ST(4);
    lds_barrier();
    ST(5);
#pragma unroll
    for (int n = 0; n < 8; ++n) acc[0][n] += *(const f32x4*)(smem + 16384 + (w ^ 2) * 8192 + l * 16 + n * 1024);
    if (LAST) {
      char* tb = smem + w * 4096;
#pragma unroll
      for (int n = 0; n < 8; ++n) *(u32x2*)(tb + c * 256 + (((2 * n + (g >> 1)) ^ c) * 16) + (g & 1) * 8) = (u32x2){pk_bf(acc[0][n][0], acc[0][n][1]), pk_bf(acc[0][n][2], acc[0][n][3])};
      char* o = out + (size_t)(p64 * 64 + 32 * rh + 16 * hp + g) * 2048 + ct * 256 + c * 16;
#pragma unroll
      for (int k = 0; k < 4; ++k) *(u32x4*)(o + k * 8192) = *(const u32x4*)(tb + (4 * k + g) * 256 + ((c ^ (4 * k + g)) * 16));
    } else {
      f32x4 p1 = zero4, p2 = zero4;
#pragma unroll
      for (int n = 0; n < 8; ++n) {
        p1 += acc[0][n];
        p2 += acc[0][n] * acc[0][n];
      }
      const float s1 = swap32_add(swap16_add((p1[0] + p1[1]) + (p1[2] + p1[3])));
      const float s2 = swap32_add(swap16_add((p2[0] + p2[1]) + (p2[2] + p2[3])));
      const unsigned eo = WS_EX + (p64 * (8 * 64) + row) * 8;
      if (l < 16) st8<16>(rw, eo + ct * 512, (u32x2){__float_as_uint(s1), __float_as_uint(s2) | 1u});
      ST(6);
      lds_barrier();
#pragma unroll
      for (int n = 0; n < 8; ++n) {
        const u32x2 y = *(const LAS u32x2*)(px + 16384 + n * 2048);
        acc[1][n] = unpack4(y);
        asm volatile("" : "+v"(acc[1][n]));
      }
      u32x2 gv[2];
      __builtin_amdgcn_s_sleep(KO_SLP0);
#pragma unroll 1
      for (unsigned spin = 0; spin < (1u << 17); ++spin) {
        const unsigned po = opaque(eo + 2 * g * 512);
#pragma unroll
        for (int i = 0; i < 2; ++i) gv[i] = __builtin_amdgcn_raw_buffer_load_b64(rw, po + i * 512, 0, 16);
        if (__all(gv[0][1] & gv[1][1] & 1)) break;
        __builtin_amdgcn_s_sleep(KO_SLP);
        asm volatile("" ::: "memory");
      }
      ST(7);
      const float mean = swap32_add(swap16_add(__uint_as_float(gv[0][0]) + __uint_as_float(gv[1][0]))) * (1.f / D_);
      const float ex2 = swap32_add(swap16_add(__uint_as_float(gv[0][1]) + __uint_as_float(gv[1][1]))) * (1.f / D_);
      const float rs = __builtin_amdgcn_rsqf(fmaxf(ex2 - mean * mean, 0.f) + 1e-5f), nm = -mean * rs;
#pragma unroll
      for (int n = 0; n < 8; ++n) {
        const f32x4 v = acc[0][n] + (acc[0][n] * rs + nm) * acc[1][n];
        st8<AUX_X3>(rw, WS_X3 + go + n * 4096, (u32x2){pk_bf(v[0], v[1]), pk_bf(v[2], v[3])});
      }
    }
  }
  ST(8);
  ST_END(LAST ? 5 : 3, ws + WS_DBG);
}

constexpr int CV_H = 4224, CV_U = 4576, CV_CH = 2 * CV_H + 2 * CV_U;
constexpr int CV_YP = 4096 + 64;
constexpr int CV_Y = 4 * CV_CH;
constexpr int CV_CS = CV_Y + 4 * CV_YP;
constexpr int CV_W = CV_CS + 1024;
constexpr int CV_P = CV_W + 32768;
constexpr int CV_LDS = CV_P + 32768 + 2048;
#define CV_LATE 0
#define CV_WEARLY 0
#define CV_HSLP 50
#define CV_XSLP 20
#define CV_WSLP 0
#define CV_SLP 1
#define CV_SPLIT 8

DI void mfma_ip(f32x4& acc, bf16x8 a, bf16x8 b) { asm volatile("v_mfma_f32_16x16x32_bf16 %0, %1, %2, %0" : "+v"(acc) : "v"(a), "v"(b)); }
template <int N0, int N1> DI void conv_tiles(const lchar* aa, const lchar* ba, bf16x8 dfrag, lchar* yb) {
  u32x2 A[2][N1][2];
  bf16x8 B[2][N1];
  f32x4 acc[N1 - N0];
  constexpr int NM = (N1 * (N1 + 1) - N0 * (N0 + 1)) / 2, NR = 3 * N1;
  constexpr int G2 = NR > NM - 4 ? NR - (NM - 4) : 0;
  auto rd = [&](int s, int o, int r) {
    const int p = r / 3, k = r % 3;
    if (k == 0) B[s][N1 - 1 - p] = *(const volatile LAS bf16x8*)(ba + 512 * (8 - N1 + p) + o);
    else A[s][p][k - 1] = *(const volatile LAS u32x2*)(aa + 512 * p + o + 8 * (k - 1));
  };
  auto step = [&](int s, int o, bool more) {
    int m = 0, r = 0;
#pragma unroll
    for (int d = 0; d < N1; ++d)
#pragma unroll
      for (int p = 0; p <= d; ++p) {
        const int dl = N1 - 1 - d, n = p + dl;
        if (n >= N0) {
          const u32x4 af = {A[s][p][0][0], A[s][p][0][1], A[s][p][1][0], A[s][p][1][1]};
          mfma_ip(acc[n - N0], __builtin_bit_cast(bf16x8, af), B[s][dl]);
          if (more && r < NR) rd(s ^ 1, o, r++);
          if (more && m < G2) rd(s ^ 1, o, r++);
          if (more && m == NM - 2) __builtin_amdgcn_s_waitcnt(0xC07F);
          ++m;
        }
      }
  };
#pragma unroll
  for (int r = 0; r < NR; ++r) rd(0, 0, r);
#pragma unroll
  for (int n = N1 - 1; n >= N0; --n) { acc[n - N0] = (f32x4){0.f, 0.f, 0.f, 0.f}; asm volatile("" : "+v"(acc[n - N0])); }
  asm volatile("s_nop 1");
#pragma unroll
  for (int n = N1 - 1; n >= N0; --n) mfma_ip(acc[n - N0], dfrag, B[0][n]);
#pragma unroll 1
  for (int it = 0; it < 3; ++it) {
    step(0, 64, true);
    step(1, 128, true);
    aa += 128; ba += 128;
  }
  step(0, 64, true);
  step(1, 0, false);
  asm volatile("s_nop 7\n\ts_nop 3");
  SB0();
#pragma unroll
  for (int n = N0; n < N1; ++n)
    *(LAS u32x2*)(yb + n * 512) = (u32x2){pk_bf(acc[n - N0][0], acc[n - N0][1]), pk_bf(acc[n - N0][2], acc[n - N0][3])};
}

extern "C" __global__ __launch_bounds__(512, 2) void k_conv(const u16* __restrict__ x0, const u16* __restrict__ kin,
                                                  const u16* __restrict__ vin, const u16* __restrict__ sk,
                                                  const u16* __restrict__ hk, const u16* __restrict__ dvec,
                                                  const u16* __restrict__ wqkv, const u16* __restrict__ wo,
                                                  char* ws) {
  extern __shared__ __attribute__((aligned(16))) char smem[];
  ST_DECL
  asm volatile("" :: "s"(x0), "s"(kin), "s"(vin), "s"(sk), "s"(hk), "s"(dvec), "s"(wqkv), "s"(wo), "s"(ws));
  const int b = blockIdx.x, w = __builtin_amdgcn_readfirstlane(threadIdx.x >> 6), l = threadIdx.x & 63;
  const int xcd = b & 7, slot = b >> 3, t = threadIdx.x & 255;
  const int quad = xcd * 32 + slot, ch = quad * 4 + (w & 3);
  const rsrc_t rw = make_rsrc(ws, WS_END);
  char* cb = smem + (w & 3) * CV_CH;
  const u32x4 z4 = {0, 0, 0, 0};
  auto next_dword = [&](const u32x4 (&c)[4], int i) {
    return (unsigned)__builtin_amdgcn_update_dpp(i < 3 ? __builtin_amdgcn_readfirstlane(c[i < 3 ? i + 1 : 0][0]) : 0, c[i][0], 0x130, 0xf, 0xf, false);
  };
  if (__builtin_expect(w < 4, 1)) {
    const int ctile = 2 * xcd + (slot & 1), rtile = slot >> 1, c8 = l & 7;
    const int ch0 = ctile * 64 + c8 * 8, sl = w * 32 + (l >> 3) * 4, s0 = rtile * 128 + sl;
    const rsrc_t rk = make_rsrc(kin, S_ * D_ * 2), rv = make_rsrc(vin, S_ * D_ * 2);
    char* ps = smem + CV_P;
    u32x4 kr[7], vr[4], wr[4];
    const u32x2 wl = *(const u32x2*)(sk + (ctile * 64 + l) * 4);
#pragma unroll
    for (int i = 0; i < 3; ++i)
      kr[i] = __builtin_amdgcn_raw_buffer_load_b128(rk, l < 8 ? (unsigned)((s0 - 3 + i) * D_ + ch0) * 2u : 0xfffffff0u, 0, NT);
#pragma unroll
    for (int i = 0; i < 4; ++i) kr[3 + i] = __builtin_amdgcn_raw_buffer_load_b128(rk, (unsigned)((s0 + i) * D_ + ch0) * 2u, 0, NT);
#pragma unroll
    for (int i = 0; i < 4; ++i) vr[i] = __builtin_amdgcn_raw_buffer_load_b128(rv, (unsigned)((s0 + i) * D_ + ch0) * 2u, 0, NT);
    unsigned tag = __builtin_amdgcn_raw_buffer_load_b32(rw, WS_NONCE, 0, 16);
    const unsigned dv = (((const unsigned*)dvec)[ch >> 1] >> ((ch & 1) * 16)) & 0xffffu;
    SB0();
    {
      char* ub = cb + 2 * CV_H;
      if (l < 30) { *(u32x4*)(ub + 4096 + l * 16) = z4; *(u32x4*)(ub + CV_U + 4096 + l * 16) = z4; }
    }
    *(u32x2*)(ps + 32768 + w * 512 + l * 8) = wl;
#pragma unroll
    for (int i = 0; i < 4; ++i) wr[i] = *(const u32x4*)(ps + 32768 + w * 512 + c8 * 64 + i * 16);
#pragma unroll
    for (int i = 0; i < 3; ++i)
#pragma unroll
      for (int j = 0; j < 4; ++j) {
        const unsigned x = __builtin_amdgcn_ds_bpermute(4 * l - 32, kr[4 + i][j]);
        kr[i][j] = l < 8 ? kr[i][j] : x;
      }
    char* lp = ps + c8 * 2048 + (((124 - sl) * 2) ^ (c8 * 16));
#pragma unroll
    for (int j = 0; j < 4; ++j) {
      const f32x2 w0 = {bf_lo(wr[j][0]), bf_lo(wr[j][2])}, w1 = {bf_hi(wr[j][0]), bf_hi(wr[j][2])};
      const f32x2 w2 = {bf_lo(wr[j][1]), bf_lo(wr[j][3])}, w3 = {bf_hi(wr[j][1]), bf_hi(wr[j][3])};
      f32x2 kf[7], u[4];
#pragma unroll
      for (int i = 0; i < 7; ++i) kf[i] = (f32x2){bf_lo(kr[i][j]), bf_hi(kr[i][j])};
#pragma unroll
      for (int i = 0; i < 4; ++i)
        u[i] = (kf[i] * w3 + kf[i + 1] * w2 + kf[i + 2] * w1 + kf[i + 3] * w0) * (f32x2){bf_lo(vr[i][j]), bf_hi(vr[i][j])};
#pragma unroll
      for (int c = 0; c < 2; ++c) *(u32x2*)(lp + (2 * j + c) * 256) = (u32x2){pk_bf(u[3][c], u[2][c]), pk_bf(u[1][c], u[0][c])};
    }
    tag += 1;
    asm volatile("" : "+v"(tag));
    ST(2);
    lds_barrier();
#pragma unroll
    for (int i = 0; i < 4; ++i) {
      const int cl = 16 * i + (t >> 4);
      const u32x4 v = *(const u32x4*)(ps + cl * 256 + (((t & 15) ^ (cl >> 3)) * 16));
      st16<16>(rw, WS_UT + (unsigned)((ctile * 64 + cl) * S_ + (S_ - 128 - rtile * 128)) * 2u + (t & 15) * 16, v);
    }
    WAIT_VM(0);
    __builtin_amdgcn_raw_buffer_store_b32(tag, rw, l == 0 ? WS_FU + ((ctile * 4 + w) * 16 + rtile) * 64 : 0xfffffff0u, 0, 16);
    ST(3);
    const int r = l & 15, g = l >> 4;
#pragma unroll 1
    for (unsigned spin = 0; spin < (1u << 17); ++spin) {
      __builtin_amdgcn_s_sleep(CV_SLP);
      const unsigned f = __builtin_amdgcn_raw_buffer_load_b32(rw, opaque(WS_FU + (((quad >> 4) * 4 + (quad & 3)) * 16 + r) * 64), 0, 16);
      asm volatile("" ::: "memory");
      if (__all(f == tag)) break;
    }
    ST(4);
    u32x4 c[4];
#pragma unroll
    for (int i = 0; i < 4; ++i) c[i] = __builtin_amdgcn_raw_buffer_load_b128(rw, WS_UT + ch * (S_ * 2) + l * 16 + i * 1024, 0, 16);
    const lchar* aa = (lchar*)cb + ((r & 1) ? 4 : CV_H) + 4 * r + 16 * g;
    const lchar* ba = (lchar*)cb + 2 * CV_H + ((r >> 3) ? 0 : CV_U) + 448 - 64 * (r & 7) + 16 * g;
    u32x4 dfr;
    const int kk = 30 - 2 * r - 8 * g;
#pragma unroll
    for (int i = 0; i < 4; ++i) dfr[i] = (kk == 2 * i) ? dv : 0u;
    lchar* yb = (lchar*)smem + CV_Y + w * CV_YP + r * 32 + ((g ^ (r >> 2)) & 3) * 8;
    asm volatile("" : "+v"(aa), "+v"(ba), "+v"(dfr), "+v"(yb));
    SB0();
#pragma unroll
    for (int i = 0; i < 4; ++i) {
      char* ub = cb + 2 * CV_H + (l + 64 * i) * 16;
      *(u32x4*)ub = c[i];
      *(u32x4*)(ub + CV_U) =
          (u32x4){__builtin_amdgcn_alignbyte(c[i][1], c[i][0], 2), __builtin_amdgcn_alignbyte(c[i][2], c[i][1], 2),
                  __builtin_amdgcn_alignbyte(c[i][3], c[i][2], 2), __builtin_amdgcn_alignbyte(next_dword(c, i), c[i][3], 2)};
    }
    lds_barrier();
    ST(5);
    conv_tiles<0, 8>(aa, ba, __builtin_bit_cast(bf16x8, dfr), yb);
    ST(6);
    lds_barrier();
    if (slot == 0 && w < 2) CODE_WARM("k_qkv", 10240, t);
  } else {
    const int hw = w - 4, r16 = quad >> 1, xc0 = (quad & 1) * 512 + hw * 128, c16 = l & 15, g = l >> 4;
    const rsrc_t rx = make_rsrc(x0, S_ * D_ * 2), rh = make_rsrc(hk, S_ * D_ * 2);
    u32x4 xr[4], c[4];
    if (t == 0) *(volatile LAS int*)((lchar*)smem + CV_CS + 512) = 0;
    __builtin_amdgcn_s_sleep(CV_HSLP);
#pragma unroll
    for (int i = 0; i < 4; ++i) c[i] = __builtin_amdgcn_raw_buffer_load_b128(rh, ch * (S_ * 2) + l * 16 + i * 1024, 0, NT);
#pragma unroll
    for (int i = 0; i < 4; ++i) xr[i] = __builtin_amdgcn_raw_buffer_load_b128(rx, (r16 * 16 + 4 * g + i) * (D_ * 2) + xc0 * 2 + c16 * 16, 0, NT);
    unsigned tag = __builtin_amdgcn_raw_buffer_load_b32(rw, WS_NONCE, 0, 16);
    const bool isq = b < 192;
    const int ks = (b & 15) * 2 + (hw & 1), cgp = ((isq ? b : b - 192) >> 4) * 4 + (hw & 2);
    const unsigned pitch = isq ? 6144 : 2048;
    const rsrc_t rwt = make_rsrc(isq ? wqkv : wo, pitch * 1024);
    unsigned vo = (ks * 32 + g) * pitch + cgp * 128 + ((c16 ^ (2 * g)) * 16);
    SB0();
    lds_barrier();
    SB0();
    ST(2);
    if (l < 4) *(u32x4*)(cb + l * 16) = z4;
    if (l < 3) *(u32x4*)(cb + CV_H + l * 16) = z4;
    if (l == 0) *(u32x4*)(cb + CV_H + 48) = (u32x4){0, 0, 0, c[0][0]};
#pragma unroll
    for (int i = 0; i < 4; ++i) {
      const int cc = l + 64 * i;
      *(u32x4*)(cb + 64 + cc * 16) = c[i];
      *(u32x4*)(cb + CV_H + 64 + cc * 16) = (u32x4){c[i][1], c[i][2], c[i][3], next_dword(c, i)};
    }
    SB0();
    __builtin_amdgcn_s_sleep(CV_XSLP);
    {
      char* xs = smem + CV_P + 16384 + hw * 4096;
#pragma unroll
      for (int p = 0; p < 2; ++p)
#pragma unroll
        for (int q = 0; q < 2; ++q)
          *(u32x4*)(xs + (2 * c16 + q) * 128 + (((2 * g + p) ^ (c16 & 7)) * 16)) =
              (u32x4){xr[2 * p][2 * q], xr[2 * p][2 * q + 1], xr[2 * p + 1][2 * q], xr[2 * p + 1][2 * q + 1]};
      const unsigned xo = WS_XQ + (unsigned)((xc0 >> 2) + (l >> 3)) * (S_ * 8) + r16 * 128 + (l & 7) * 16;
#pragma unroll
      for (int i = 0; i < 4; ++i) {
        const int q = 8 * i + (l >> 3);
        st16<16>(rw, xo + i * (8 * S_ * 8), *(const u32x4*)(xs + q * 128 + (((l & 7) ^ ((q >> 1) & 7)) * 16)));
      }
      f32x2* sp = (f32x2*)(smem + CV_CS);
#pragma unroll
      for (int i = 0; i < 4; ++i) {
        f32x2 s1 = {0.f, 0.f}, s2 = s1;
#pragma unroll
        for (int j = 0; j < 4; ++j) {
          const f32x2 v = {bf_lo(xr[i][j]), bf_hi(xr[i][j])};
          s1 += v;
          s2 += v * v;
        }
        float t1 = s1[0] + s1[1], t2 = s2[0] + s2[1];
        t1 += dpp_mov<0xB1>(t1); t2 += dpp_mov<0xB1>(t2);
        t1 += dpp_mov<0x4E>(t1); t2 += dpp_mov<0x4E>(t2);
        t1 += dpp_mov<0x141>(t1); t2 += dpp_mov<0x141>(t2);
        t1 += dpp_mov<0x140>(t1); t2 += dpp_mov<0x140>(t2);
        if (c16 == 0) sp[hw * 16 + 4 * g + i] = (f32x2){t1, t2};
      }
    }
    tag += 1;
    asm volatile("" : "+v"(tag), "+v"(vo));
    {
      asm volatile("s_waitcnt lgkmcnt(0)" ::: "memory");
      int old = 0;
      if (l == 0) old = __hip_atomic_fetch_add((LAS int*)((lchar*)smem + CV_CS + 512), 1, __ATOMIC_RELAXED, __HIP_MEMORY_SCOPE_WORKGROUP);
      if (__builtin_amdgcn_readfirstlane(old) == 3) {
        asm volatile("" ::: "memory");
        const f32x2* sp = (const f32x2*)(smem + CV_CS) + c16;
        const f32x2 v = (sp[0] + sp[16]) + (sp[32] + sp[48]);
        st8<16>(rw, l < 16 ? WS_ST0 + (quad & 1) * 16384 + (r16 * 16 + l) * 8 : 0xfffffff0u, __builtin_bit_cast(u32x2, v));
      }
    }
    WAIT_VM(0);
    __builtin_amdgcn_raw_buffer_store_b32(tag, rw, l == 0 ? WS_FX + (quad * 4 + hw) * 4 : 0xfffffff0u, 0, 16);
    ST(3);
    lds_barrier();
    SB0();
    __builtin_amdgcn_s_sleep(CV_WSLP);
#pragma unroll
    for (int i = 0; i < 8; ++i)
      blds_n<1, 1>(rwt, (i & 1) ? vo ^ 128u : vo, i * 4 * pitch, (unsigned)(size_t)(LAS char*)smem + CV_W + hw * 8192 + i * 1024);

#pragma unroll 1
    for (unsigned spin = 0; spin < (1u << 17); ++spin) {
      const u32x4 f = __builtin_amdgcn_raw_buffer_load_b128(rw, opaque(WS_FX + ((4 * (l >> 4) + hw) * 16 + (l & 15)) * 16), 0, 16);
      asm volatile("" ::: "memory");
      if (__all(f[0] == tag && f[1] == tag && f[2] == tag && f[3] == tag)) break;
      __builtin_amdgcn_s_sleep(2);
    }
    u32x4 xq[4];
    f32x4 st[4], st2[4];
#pragma unroll
    for (int i = 0; i < 4; ++i) {
      st2[i] = __builtin_bit_cast(f32x4, __builtin_amdgcn_raw_buffer_load_b128(rw, WS_ST0 + hw * 1024 + l * 16, 16384 + i * 4096, 16));
      xq[i] = __builtin_amdgcn_raw_buffer_load_b128(rw, WS_XQ + quad * (S_ * 8) + hw * 1024 + l * 16, i * 4096, 16);
      st[i] = __builtin_bit_cast(f32x4, __builtin_amdgcn_raw_buffer_load_b128(rw, WS_ST0 + hw * 1024 + l * 16, i * 4096, 16));
    }
    ST(4);
    SB0();
    bf16x8 fg[8];
    {
      char* wb = smem + CV_W + hw * 8192;
      const int q4 = (l >> 2) & 3;
      const int rd = (4 * g + q4) * 256 + (l & 3) * 8, fr = ((4 * g + q4) & 7) * 32;
#pragma unroll
      for (int i = 0; i < 8; ++i) {
        const char* p = wb + rd + ((i * 32) ^ fr);
        fg[i] = lds_tr2(p, p + 4096);
      }
    }
    {
      const unsigned o = (isq ? WS_WQ + (unsigned)((cgp & 15) * 32 + ks) * 12288u + (cgp >> 4) * 4096u
                              : WS_WO + (unsigned)(cgp * 32 + ks) * 4096u) + l * 16;
      const unsigned o2 = isq ? 32u * 12288u : 32u * 4096u;
#pragma unroll
      for (int i = 0; i < 8; ++i) st16<AUX_W>(rw, o + (i >> 2) * o2 + (i & 3) * 1024, __builtin_bit_cast(u32x4, fg[i]));
      st16<16>(rw, t < 160 ? WS_ZERO + (quad * 160 + t) * 16 : 0xfffffff0u, z4);
#pragma unroll
      for (int i = 0; i < 3; ++i) st16<16>(rw, t + 256 * i < 528 ? WS_KVP + (b * 528 + t + 256 * i) * 16 : 0xfffffff0u, z4);
    }
    SB0();
    f32x4 xf[4][2], xn[4][2];
#pragma unroll
    for (int i = 0; i < 4; ++i)
#pragma unroll
      for (int r = 0; r < 2; ++r) {
        xf[i][r] = unpack4((u32x2){xq[i][2 * r], xq[i][2 * r + 1]});
        float mean = st[i][2 * r] + st2[i][2 * r], ex2 = st[i][2 * r + 1] + st2[i][2 * r + 1];
        asm("" : "+v"(mean));
        mean *= 1.f / D_;
        asm("" : "+v"(mean));
        const float rs = __builtin_amdgcn_rsqf(fmaxf(ex2 * (1.f / D_) - mean * mean, 0.f) + 1e-5f), nm = -mean * rs;
#pragma unroll
        for (int e = 0; e < 4; ++e)
          asm("v_fma_f32 %0, %1, %2, %3" : "=v"(xn[i][r][e]) : "v"(xf[i][r][e]), "v"(rs), "v"(nm));
        asm volatile("" : "+v"(xf[i][r]), "+v"(xn[i][r]));
      }
    const int ya = 4 * (hw & 1) + (l >> 4);
    const char* yp = smem + CV_Y + ((l >> 2) & 3) * CV_YP + (hw >> 1) * 512 + ya * 32;
    const int y0 = ((l ^ (ya >> 2)) & 3) * 8, y1 = 256 + ((l ^ (2 + (ya >> 2))) & 3) * 8;
    const unsigned go = (unsigned)hw * 262144 + quad * 1024 + ((l * 16) ^ ((quad & 1) * 128));
    auto fin = [&](int i) {
      const u32x4 y = __builtin_bit_cast(u32x4, lds_tr2(yp + i * 1024 + y0, yp + i * 1024 + y1));
      st16<AUX_X1>(rw, WS_Y + go + i * 1048576, y);
      const f32x4 o0 = xf[i][0] + xn[i][0] * unpack4((u32x2){y[0], y[1]}), o1 = xf[i][1] + xn[i][1] * unpack4((u32x2){y[2], y[3]});
      st16<AUX_X1>(rw, WS_X1 + go + i * 1048576, __builtin_bit_cast(u32x4, pack8(o0, o1)));
    };
    ST(5);
    lds_barrier();
    if (b == 0 && w == 4) __builtin_amdgcn_raw_buffer_store_b32(tag, rw, l == 0 ? WS_NONCE : 0xfffffff0u, 0, 16);
#pragma unroll
    for (int i = 0; i < 4; ++i) fin(i);
    ST(7);
    if (isq) {
      const u32x4 one4 = {0x3f803f80u, 0x3f803f80u, 0x3f803f80u, 0x3f803f80u};
      float* red = (float*)(smem + CV_P);
#pragma unroll
      for (int i = 0; i < 8; ++i) {
        const f32x4 cs = MFMA16(__builtin_bit_cast(bf16x8, one4), fg[i], ((f32x4){0.f, 0.f, 0.f, 0.f}));
        if (l < 16) red[hw * 128 + i * 16 + l] = cs[0];
      }
      lds_barrier();
      const int e = (t >> 7) * 256 + (t & 127);
      ((float*)(ws + WS_CS))[(b & 15) * 3072 + (b >> 4) * 256 + t] = red[e] + red[e + 128];
    }
  }
  ST(8);
  ST_END(1, ws + WS_DBG);
}

extern "C" void kernel_launch(void* const* d_in, const int* in_sizes, int n_in, void* d_out, int out_size, void* d_ws,
                              size_t ws_size, hipStream_t stream) {
  const u16* x0 = (const u16*)d_in[0];
  const u16* kin = (const u16*)d_in[1];
  const u16* vin = (const u16*)d_in[2];
  const u16* sk = (const u16*)d_in[3];
  const u16* hk = (const u16*)d_in[4];
  const u16* dv = (const u16*)d_in[5];
  const u16* wqkv = (const u16*)d_in[7];
  const u16* wo = (const u16*)d_in[8];
  char* ws = (char*)d_ws;
  (void)hipFuncSetAttribute((const void*)k_conv, hipFuncAttributeMaxDynamicSharedMemorySize, CV_LDS);
  (void)hipFuncSetAttribute((const void*)k_qkv, hipFuncAttributeMaxDynamicSharedMemorySize, QK_LDS);
  (void)hipFuncSetAttribute((const void*)k_out, hipFuncAttributeMaxDynamicSharedMemorySize, KO_LDS);
  k_conv<<<256, 512, CV_LDS, stream>>>(x0, kin, vin, sk, hk, dv, wqkv, wo, ws);
  k_qkv<<<256, 512, QK_LDS, stream>>>(ws, WS_X1, 0);
  k_out<<<256, 512, KO_LDS, stream>>>(ws, (char*)d_out, 0);
  k_qkv<<<256, 512, QK_LDS, stream>>>(ws, WS_X3, 1);
  k_out<<<256, 512, KO_LDS, stream>>>(ws, (char*)d_out, 1);
}
```

```cpp
#include <hip/hip_runtime.h>
#include <stdint.h>

typedef unsigned short u16;
typedef __bf16 bf16x2 __attribute__((ext_vector_type(2)));
typedef __bf16 bf16x4 __attribute__((ext_vector_type(4)));
typedef __bf16 bf16x8 __attribute__((ext_vector_type(8)));
typedef float f32x2 __attribute__((ext_vector_type(2)));
typedef float f32x4 __attribute__((ext_vector_type(4)));
typedef unsigned u32x2 __attribute__((ext_vector_type(2)));
typedef unsigned u32x4 __attribute__((ext_vector_type(4)));
typedef __amdgpu_buffer_rsrc_t rsrc_t;
#define LAS __attribute__((address_space(3)))
#define DI __device__ __forceinline__
typedef LAS char lchar;

#define ST_DECL
#define ST(i)
#define ST_END(k, dbg)
#define ST_FLUSH(k, dbg)
constexpr size_t WS_DBG = 64u << 20;

constexpr int S_ = 2048, D_ = 1024;
constexpr unsigned WS_UT = 0;
constexpr unsigned WS_X1 = 4u << 20;
constexpr unsigned WS_Y = 8u << 20;
constexpr unsigned WS_X3 = 12u << 20;
constexpr unsigned WS_PQ = 16u << 20;
constexpr unsigned WS_WQ = 20u << 20;
constexpr unsigned WS_WO = 26u << 20;
constexpr unsigned WS_XQ = 28u << 20;
constexpr unsigned WS_ST0 = 32u << 20;
constexpr unsigned WS_CS = WS_ST0 + 65536;
constexpr unsigned WS_KVF = WS_CS + 196608;
constexpr unsigned KVF_BYTES = 65536 + 2048;
constexpr unsigned WS_ZERO = WS_KVF + 2 * KVF_BYTES;
constexpr unsigned WS_EX = WS_ZERO;
constexpr unsigned WS_RS = WS_EX + 32 * 8 * 64 * 8;
constexpr unsigned WS_F2 = WS_RS + 2 * 262144;
constexpr unsigned WS_FQ = WS_F2 + 65536;
constexpr unsigned WS_F3 = WS_FQ + 32768;
constexpr unsigned ZERO_BYTES = WS_F3 + 16384 - WS_ZERO;
static_assert(ZERO_BYTES == 256 * 188 * 16, "zeroed region");
constexpr unsigned WS_KVP = 34u << 20;
constexpr unsigned KVP_BLK = 2112 * 4;
constexpr unsigned WS_NONCE = 39u << 20;
constexpr unsigned WS_FU = WS_NONCE + 4096;
constexpr unsigned WS_FX = WS_FU + 65536;
constexpr unsigned WS_END = 40u << 20;
constexpr int NT = 2;

#define AUX_UT 16
#define AUX_XQ 0
#define AUX_X1 16
#define AUX_Z 16
#define AUX_W 16
#define AUX_PQ 16
#define AUX_X3 16

extern "C" __device__ float mul_nopk(float, float) __asm("llvm.amdgcn.fmul.legacy");
DI float bf_lo(unsigned u) { return __uint_as_float(u << 16); }
DI float bf_hi(unsigned u) { return __uint_as_float(u & 0xffff0000u); }
DI unsigned pk_bf(float lo, float hi) {
  f32x2 v = {lo, hi};
  return __builtin_bit_cast(unsigned, __builtin_convertvector(v, bf16x2));
}
template <int CTRL> DI float dpp_mov(float x) {
  return __int_as_float(__builtin_amdgcn_update_dpp(0, __float_as_int(x), CTRL, 0xf, 0xf, true));
}
DI float swap32_add(float x) {
  auto r = __builtin_amdgcn_permlane32_swap(__float_as_uint(x), __float_as_uint(x), false, false);
  return __uint_as_float(r[0]) + __uint_as_float(r[1]);
}
DI float swap16_add(float x) {
  auto r = __builtin_amdgcn_permlane16_swap(__float_as_uint(x), __float_as_uint(x), false, false);
  return __uint_as_float(r[0]) + __uint_as_float(r[1]);
}
DI float wave_sum(float x) {
  x += dpp_mov<0xB1>(x);
  x += dpp_mov<0x4E>(x);
  x += dpp_mov<0x141>(x);
  x += dpp_mov<0x140>(x);
  return swap32_add(swap16_add(x));
}
DI void lds_barrier() {
  asm volatile("" ::: "memory");
  __builtin_amdgcn_s_waitcnt(0xC07F);
  __builtin_amdgcn_s_barrier();
  asm volatile("" ::: "memory");
}
#define SB0() __builtin_amdgcn_sched_barrier(0)
#define SGB(mask, n) __builtin_amdgcn_sched_group_barrier(mask, n, 0)
#define WAIT_VM(n) asm volatile("s_waitcnt vmcnt(%0)" :: "n"(n) : "memory")
#define MFMA16(a, b, c) __builtin_amdgcn_mfma_f32_16x16x32_bf16((a), (b), (c), 0, 0, 0)
DI void mfma_ip(f32x4& acc, bf16x8 a, bf16x8 b) { asm volatile("v_mfma_f32_16x16x32_bf16 %0, %1, %2, %0" : "+v"(acc) : "v"(a), "v"(b)); }
DI bf16x8 lds_b128(const char* p) { return *(const bf16x8*)p; }
DI bf16x8 lds_b128(const lchar* p) { return *(const LAS bf16x8*)p; }
DI bf16x8 lds_tr2(const char* p0, const char* p1) {
  bf16x4 a = __builtin_amdgcn_ds_read_tr16_b64_v4bf16((LAS bf16x4*)p0);
  bf16x4 b = __builtin_amdgcn_ds_read_tr16_b64_v4bf16((LAS bf16x4*)p1);
  return __builtin_shufflevector(a, b, 0, 1, 2, 3, 4, 5, 6, 7);
}
DI bf16x8 lds_2b64(const lchar* p0, const lchar* p1) {
  const u32x2 a = *(const volatile LAS u32x2*)p0, b = *(const volatile LAS u32x2*)p1;
  const u32x4 r = {a[0], a[1], b[0], b[1]};
  return __builtin_bit_cast(bf16x8, r);
}
DI f32x4 unpack4(u32x2 v) { return (f32x4){bf_lo(v[0]), bf_hi(v[0]), bf_lo(v[1]), bf_hi(v[1])}; }
DI bf16x8 pack8(f32x4 a, f32x4 b) {
  u32x4 p = {pk_bf(a[0], a[1]), pk_bf(a[2], a[3]), pk_bf(b[0], b[1]), pk_bf(b[2], b[3])};
  return __builtin_bit_cast(bf16x8, p);
}
DI rsrc_t make_rsrc(const void* p, unsigned bytes) { return __builtin_amdgcn_make_buffer_rsrc((void*)p, (short)0, (int)bytes, 0x00020000); }
#define BLDS_(o, p) "buffer_load_dwordx4 %1, %2, %3 offen offset:" #o p " lds\n\t"
#define BLDS_ASM(str) asm volatile("s_mov_b32 m0, %0\n\ts_nop 0\n\t" str :: "s"(lds_base), "v"(voff), "s"(rsrc), "s"(soff) : "memory")
#define BLDS_ALL(p)                                                                       \
  if constexpr (N == 1) BLDS_ASM(BLDS_(0, p));                                            \
  else if constexpr (N == 2) BLDS_ASM(BLDS_(0, p) BLDS_(1024, p));                        \
  else if constexpr (N == 3) BLDS_ASM(BLDS_(0, p) BLDS_(1024, p) BLDS_(2048, p));         \
  else BLDS_ASM(BLDS_(0, p) BLDS_(1024, p) BLDS_(2048, p) BLDS_(3072, p));
template <int N, int P = 0> DI void blds_n(rsrc_t rsrc, unsigned voff, unsigned soff, unsigned lds_base) {
  if constexpr (P == 0) { BLDS_ALL("") } else if constexpr (P == 1) { BLDS_ALL(" nt") } else { BLDS_ALL(" sc1") }
}
#define POL_A 0
#define POL_W 0
#define CODE_WARM(sym, bytes, t)                                                                                        \
  do {                                                                                                                  \
    unsigned lo_, hi_;                                                                                                  \
    asm volatile("s_getpc_b64 s[40:41]\n\ts_add_u32 %0, s40, " sym "@rel32@lo+4\n\ts_addc_u32 %1, s41, " sym "@rel32@hi+12" \
                 : "=&s"(lo_), "=&s"(hi_) :: "scc", "s40", "s41");                                                      \
    if ((t) * 128 < (bytes)) {                                                                                          \
      const unsigned v_ = *(const volatile __attribute__((address_space(1))) unsigned*)((((size_t)hi_ << 32) | lo_) + (t) * 128); \
      asm volatile("" :: "v"(v_));                                                                                      \
    }                                                                                                                   \
  } while (0)
DI unsigned opaque(unsigned v) { asm volatile("" : "+v"(v) :: "memory"); return v; }
template <int AUX> DI void st8(rsrc_t r, unsigned voff, u32x2 v) { __builtin_amdgcn_raw_buffer_store_b64(v, r, voff, 0, AUX); }
template <int AUX> DI void st16(rsrc_t r, unsigned voff, u32x4 v) { __builtin_amdgcn_raw_buffer_store_b128(v, r, voff, 0, AUX); }

#define QK_SLP0 16
constexpr int QK_STAGE = 40960, QK_LDS = 4 * QK_STAGE;
constexpr int QK_RS = 0;
constexpr int QK_KV = QK_RS + 2048;
#define QK_P 7
constexpr float LOG2E = 1.44269504088896f, LN2 = 0.693147180559945f;

DI f32x4 phi4(f32x4 u) {
  f32x4 e, m;
#pragma unroll
  for (int i = 0; i < 4; ++i) {
    e[i] = __builtin_amdgcn_fmed3f(__builtin_amdgcn_exp2f(u[i]), 0.f, 1.f);
    m[i] = fmaxf(u[i], 1e-6f * LOG2E);
  }
  return m * LN2 + e;
}

#define KO_SLP 1
#define KO_SLP0 4
constexpr int KO_STAGE = 28672;
constexpr int KO_Y = 0, KO_X = 16384;
constexpr int KO_R = 32768;
constexpr int KO_Z = KO_R + 4 * KO_STAGE;
#define G2SLP 8
#define PSLP 1
template <class F> DI void poll(F ok) {
#pragma unroll 1
  for (unsigned spin = 0; spin < (1u << 17); ++spin) {
    const bool r = ok();
    asm volatile("" ::: "memory");
    if (__all(r)) break;
    __builtin_amdgcn_s_sleep(PSLP);
  }
}

#define PHASE_IDS                                                                                                        \
  int b = blockIdx.x, w = w0, l = threadIdx.x & 63;                                                                      \
  unsigned lbase = (unsigned)(size_t)(LAS char*)smem;                                                                    \
  asm volatile("" : "+s"(b), "+s"(w), "+s"(lbase));                                                                      \
  asm volatile("" : "+v"(l));                                                                                             \
  const int xcd = b & 7, slot = b >> 3, lw = w & 3, c = l & 15, g = l >> 4, t = lw * 64 + l;                             \
  const int panel = 8 * (xcd & 1) + (slot & 7), ct = 4 * (xcd >> 1) + (slot >> 3);     \
  const int p64 = 8 * (xcd & 3) + (slot & 7), oc = 4 * (xcd >> 2) + (slot >> 3);      \
              \
  const unsigned va = (layer ? WS_X3 : WS_X1) + panel * 262144 + lw * 2048 + l * 16;                                     \
  const unsigned vb = WS_WQ + ct * (32 * 12288) + lw * 3072 + l * 16;                                                    \
  auto kstep = [&](int tt, int h) {                                                                                      \
    const int o = tt < QK_P ? tt : tt - 1;                                                                               \
    return __builtin_amdgcn_readfirstlane(2 * (tt == QK_P ? ct : (o < ct ? o : o + 1)) + h);                                                           \
  };                                                                                                                     \
  auto issue_a = [&](int tt, int sl, int h) { blds_n<2, 2>(rw, va, kstep(tt, h) * 8192, lbase + sl * QK_STAGE + (4 * h + lw) * 2048); }; \
  auto issue_b = [&](int tt, int sl, int h) { blds_n<3>(rw, vb, kstep(tt, h) * 12288, lbase + sl * QK_STAGE + 16384 + h * 12288 + lw * 3072); }; \
  auto issue = [&](int tt, int sl, int h) { issue_a(tt, sl, h); issue_b(tt, sl, h); };                                   \
  const unsigned vq = WS_PQ + (p64 >> 1) * (32 * 8192) + (lw >> 1) * 8192 + (p64 & 1) * 4096 + (lw & 1) * 2048 + l * 16; \
  const unsigned vw = WS_WO + (2 * oc + (lw >> 1)) * (1024 * 128) + (lw & 1) * 4096 + l * 16;                            \
  const unsigned kvf = WS_KVF + layer * KVF_BYTES, vk = kvf + lw * 1024 + l * 16;                                        \
  auto stage_q = [&](int u) { blds_n<2, 2>(rw, vq, u * 16384, lbase + KO_R + (u & 3) * KO_STAGE + lw * 2048); };         \
  auto stage_w = [&](int u) { blds_n<4>(rw, vw, u * 8192, lbase + KO_R + (u & 3) * KO_STAGE + 8192 + lw * 4096); };      \
  const unsigned rsoff = WS_RS + layer * 262144 + panel * 16384;                                                         \
  (void)c; (void)g; (void)t; (void)issue; (void)stage_q; (void)stage_w; (void)vk; (void)rsoff;

extern "C" __global__ __launch_bounds__(512, 2) void k_main(char* ws, char* __restrict__ out) {
  extern __shared__ __attribute__((aligned(16))) char smem[];
  ST_DECL
  const int w0 = __builtin_amdgcn_readfirstlane(threadIdx.x >> 6);
  const rsrc_t rw = make_rsrc(ws, WS_END);
  float* s_rs = (float*)(smem + QK_RS);
  float* s_cs = s_rs + 256;
  float* s_kv = (float*)(smem + QK_KV);
  {
    const int layer = 0;
    PHASE_IDS
    issue(0, 0, w >> 2);
  }
  if (w0 >= 4) {
#pragma unroll 1
    for (int layer = 0; layer < 2; ++layer) {
     {
      PHASE_IDS
      unsigned ro = rsoff + 4 * g * 1024 + (32 * lw + 2 * c) * 8;
      unsigned co = t < 192 ? WS_CS + (4 * g * 3072 + lw * 1024 + ct * 64 + c * 4) * 4 : 0xfffffff0u;
      LAS float* wrs = (LAS float*)s_rs + (g >> 1) * 128 + 32 * lw + 2 * c + (g & 1);
      LAS float* wcs = (LAS float*)s_cs + lw * 64 + c * 4 + g;
      if (layer == 0) {
        issue(1, 1, 0);
        asm volatile("" : "+v"(ro), "+v"(co), "+v"(wrs), "+v"(wcs));
        WAIT_VM(5);
        __builtin_amdgcn_s_barrier();
        issue(1, 1, 1); issue(2, 2, 0); issue(2, 2, 1);
        WAIT_VM(10);
        __builtin_amdgcn_s_barrier();
        issue(3, 3, 0); issue(3, 3, 1); issue(4, 0, 0);
        WAIT_VM(15);
      } else {
        asm volatile("" : "+v"(ro), "+v"(co), "+v"(wrs), "+v"(wcs));
        if (lw == 0)
          poll([&]() { return __builtin_amdgcn_raw_buffer_load_b32(rw, opaque(WS_F3 + (panel * 64 + l) * 16), 0, 16) != 0; });
        ST(2);
        __builtin_amdgcn_s_barrier();
        issue_a(0, 0, 1);
#pragma unroll
        for (int i = 2; i < 8; ++i) issue_a(i >> 1, i >> 1, i & 1);
        WAIT_VM(12);
        __builtin_amdgcn_s_barrier();
        WAIT_VM(8);
        __builtin_amdgcn_s_barrier();
        issue(4, 0, 0);
        WAIT_VM(9);
      }
      __builtin_amdgcn_s_barrier();
      for (int u = 2; u < 13; ++u) {
        issue(u + 2, (u + 2) & 3, 1); issue(u + 3, (u + 3) & 3, 0);
        WAIT_VM(15);
        __builtin_amdgcn_s_barrier();
      }
      issue(15, 3, 1);
      u32x4 xr[4];
#pragma unroll
      for (int i = 0; i < 4; ++i) xr[i] = __builtin_amdgcn_raw_buffer_load_b128(rw, ro, i * 1024, 16);
      u32x4 cr[4];
#pragma unroll
      for (int i = 0; i < 4; ++i) cr[i] = __builtin_amdgcn_raw_buffer_load_b128(rw, co, i * 12288, 0);
      SB0();
      WAIT_VM(18);
      __builtin_amdgcn_s_barrier();
      SB0();
      __builtin_amdgcn_s_setprio(2);
#pragma unroll 1
      for (unsigned spin = 0; spin < (1u << 17); ++spin) {
        if (__all(xr[0][1] & xr[0][3] & xr[1][1] & xr[1][3] & xr[2][1] & xr[2][3] & xr[3][1] & xr[3][3] & 1)) break;
        __builtin_amdgcn_s_sleep(1);
        ro = opaque(ro);
#pragma unroll
        for (int i = 0; i < 4; ++i) xr[i] = __builtin_amdgcn_raw_buffer_load_b128(rw, ro, i * 1024, 16);
        asm volatile("" ::: "memory");
      }
      __builtin_amdgcn_s_waitcnt(0x0F70);
      SB0();
      {
        float p[4], c0[4];
#pragma unroll
        for (int e = 0; e < 4; ++e) {
          float a = __uint_as_float(xr[0][e]) + __uint_as_float(xr[1][e]), d = __uint_as_float(xr[2][e]) + __uint_as_float(xr[3][e]);
          asm("" : "+v"(a));
          p[e] = a + d;
          asm("" : "+v"(p[e]));
          a = __uint_as_float(cr[0][e]) + __uint_as_float(cr[1][e]); d = __uint_as_float(cr[2][e]) + __uint_as_float(cr[3][e]);
          asm("" : "+v"(a));
          c0[e] = a + d;
          asm("" : "+v"(c0[e]));
        }
        asm volatile("" : "+v"(p[0]), "+v"(p[1]), "+v"(p[2]), "+v"(p[3]), "+v"(c0[0]), "+v"(c0[1]), "+v"(c0[2]), "+v"(c0[3]));
        SB0();
        __builtin_amdgcn_s_barrier();
        SB0();
        auto r0 = __builtin_amdgcn_permlane16_swap(__float_as_uint(p[0]), __float_as_uint(p[2]), false, false);
        auto r1 = __builtin_amdgcn_permlane16_swap(__float_as_uint(p[1]), __float_as_uint(p[3]), false, false);
        const float mean = swap32_add(__uint_as_float(r0[0]) + __uint_as_float(r0[1])) * (1.f / D_);
        const float ex2 = swap32_add(__uint_as_float(r1[0]) + __uint_as_float(r1[1])) * (1.f / D_);
        const float rs = __builtin_amdgcn_rsqf(fmaxf(ex2 - mean * mean, 0.f) + 1e-5f) * LOG2E;
        *wrs = (g >> 1) ? -mean * rs : rs;
        auto q0 = __builtin_amdgcn_permlane16_swap(__float_as_uint(c0[0]), __float_as_uint(c0[1]), false, false);
        auto q1 = __builtin_amdgcn_permlane16_swap(__float_as_uint(c0[2]), __float_as_uint(c0[3]), false, false);
        auto q2 = __builtin_amdgcn_permlane32_swap(__float_as_uint(__uint_as_float(q0[0]) + __uint_as_float(q0[1])),
                                                   __float_as_uint(__uint_as_float(q1[0]) + __uint_as_float(q1[1])), false, false);
        *wcs = __uint_as_float(q2[0]) + __uint_as_float(q2[1]);
      }
    ST(3);
      lds_barrier();
#pragma unroll
      for (int u = 0; u < 4; ++u) stage_w(u);
      ST(4);
      lds_barrier();
      ST(5);
      const unsigned pb = WS_KVP + (layer * 16 + ct) * (16 * KVP_BLK);
#pragma unroll
      for (int i = 0; i < 3; ++i) {
        const int e = t + 256 * i;
        u32x4 v = __builtin_bit_cast(u32x4, *(const f32x4*)(s_kv + e * 4) + *(const f32x4*)(s_kv + 2112 + e * 4));
        v[1] |= 1u; v[3] |= 1u;
        st16<16>(rw, e < 528 ? pb + panel * KVP_BLK + e * 16 : 0xfffffff0u, v);
      }
      ST(6);
      __builtin_amdgcn_s_sleep(QK_SLP0);
      if (lw == 0) {
        unsigned go = pb + (8 * (l >> 5)) * KVP_BLK + panel * 512 + (l & 31) * 16;
        u32x4 v[8];
#pragma unroll 1
        for (unsigned spin = 0; spin < (1u << 17); ++spin) {
          unsigned ok = 1;
          go = opaque(go);
#pragma unroll
          for (int i = 0; i < 8; ++i) {
            v[i] = __builtin_amdgcn_raw_buffer_load_b128(rw, go, i * KVP_BLK, 16);
            ok &= v[i][1] & v[i][3];
          }
          if (__all(ok)) break;
          asm volatile("" ::: "memory");
        }
        f32x4 a = __builtin_bit_cast(f32x4, v[0]);
#pragma unroll
        for (int i = 1; i < 8; ++i) a += __builtin_bit_cast(f32x4, v[i]);
#pragma unroll
        for (int e = 0; e < 4; ++e) a[e] = swap32_add(a[e]);
        st8<16>(rw, l < 32 ? kvf + ct * 4096 + panel * 256 + l * 8 : 0xfffffff0u, (u32x2){pk_bf(a[0], a[1]), pk_bf(a[2], a[3])});
      } else if (lw == 1 && panel < 8) {
        const unsigned go = pb + ((l >> 1) & 15) * KVP_BLK + 8192 + panel * 32 + (l & 1) * 16;
        u32x4 v;
#pragma unroll 1
        for (unsigned spin = 0; spin < (1u << 17); ++spin) {
          v = __builtin_amdgcn_raw_buffer_load_b128(rw, opaque(go), 0, 16);
          if (__all(v[1] & v[3] & 1)) break;
          asm volatile("" ::: "memory");
        }
        f32x4 a = __builtin_bit_cast(f32x4, v);
#pragma unroll
        for (int e = 0; e < 4; ++e) {
          a[e] += dpp_mov<0x4E>(a[e]);
          a[e] += dpp_mov<0x124>(a[e]);
          a[e] += dpp_mov<0x128>(a[e]);
          a[e] = swap16_add(a[e]);
        }
        st8<16>(rw, l < 2 ? kvf + 65536 + ct * 128 + panel * 16 + l * 8 : 0xfffffff0u, (u32x2){pk_bf(a[0], a[1]), pk_bf(a[2], a[3])});
      }
    ST(7);
      WAIT_VM(0);
      if (lw < 2)
        __builtin_amdgcn_raw_buffer_store_b32(1u, rw, l < 8 ? WS_F2 + ((layer * 8 + l) * 256 + b) * 16 + lw * 4 : 0xfffffff0u, 0, 16);
      __builtin_amdgcn_s_setprio(0);
      ST_FLUSH(2 + 2 * layer, ws + WS_DBG);
     }
      PHASE_IDS
      const unsigned vx = (p64 >> 1) * 262144 + (oc * 32 + 8 * lw + (l >> 5)) * 1024 + (p64 & 1) * 512 + (l & 31) * 16;
      auto stage_k = [&](int u) { blds_n<1, 2>(rw, vk, u * 4096, lbase + KO_R + (u & 3) * KO_STAGE + 24576 + lw * 1024); };
      auto stage = [&](int u) { stage_k(u); stage_q(u); stage_w(u); };
      auto xtile = [&](unsigned off, int o) {
#pragma unroll
        for (int i = 0; i < 4; ++i) blds_n<1, 2>(rw, vx, off + i * 2048, lbase + o + (4 * lw + i) * 1024);
      };
      __builtin_amdgcn_s_barrier();
      __builtin_amdgcn_s_barrier();
      ST(2);
      stage_k(0);
#pragma unroll
      for (int u = 1; u < 4; ++u) { stage_k(u); stage_q(u); }
      WAIT_VM(9);
      __builtin_amdgcn_s_barrier();
      WAIT_VM(6);
      __builtin_amdgcn_s_barrier();
      stage(4);
      WAIT_VM(10);
      __builtin_amdgcn_s_barrier();
      for (int u = 2; u < 13; ++u) {
        stage(u + 3);
        WAIT_VM(14);
        __builtin_amdgcn_s_barrier();
      }
      xtile(layer ? WS_X3 : WS_X1, KO_X);
      WAIT_VM(11);
      __builtin_amdgcn_s_barrier();
      if (layer) {
        WAIT_VM(4);
        __builtin_amdgcn_s_barrier();
        WAIT_VM(0);
        __builtin_amdgcn_s_barrier();
        __builtin_amdgcn_s_barrier();
      } else {
        xtile(WS_Y, KO_Y);
        WAIT_VM(8);
        __builtin_amdgcn_s_barrier();
        WAIT_VM(4);
        __builtin_amdgcn_s_barrier();
        __builtin_amdgcn_s_barrier();
        WAIT_VM(0);
        __builtin_amdgcn_s_barrier();
        __builtin_amdgcn_s_barrier();
#pragma unroll
        for (int i = 0; i < 8; ++i) issue_b(i >> 1, i >> 1, i & 1);
      }
      ST_FLUSH(3 + 2 * layer, ws + WS_DBG);
    }
  } else {
#pragma unroll 1
    for (int layer = 0; layer < 2; ++layer) {
      {
        PHASE_IDS
        const int wr = w >> 1, wh = w & 1;
        f32x4 acc[6][4];
#pragma unroll
        for (int j = 0; j < 6; ++j)
#pragma unroll
          for (int i = 0; i < 4; ++i) { acc[j][i] = (f32x4){0.f, 0.f, 0.f, 0.f}; asm volatile("" : "+v"(acc[j][i])); }
        const lchar* qa = (lchar*)smem + g * 1024 + (64 * wr + c) * 8 + (g & 1) * 128;
        const lchar* qa2 = qa + ((g & 1) ? -128 : 128);
        const lchar* qb = (lchar*)smem + 16384 + wh * 2048 + l * 16;
        u32x2 fa[2][4][2];
        bf16x8 fb[2][6];
        auto rd = [&](int buf, int h, int r) {
          if (r == 2) fb[buf][0] = *(const volatile LAS bf16x8*)(qb + h * 12288);
          else if (r < 9) {
            const int k = r < 2 ? r : r - 1, i = k >> 1;
            fa[buf][i][k & 1] = *(const volatile LAS u32x2*)(((i & 1) ? qa2 : qa) + h * 8192 + (i >> 1) * 256 + (k & 1) * 4096);
          } else fb[buf][r - 8] = *(const volatile LAS bf16x8*)(qb + h * 12288 + ((r - 8) >> 1) * 4096 + ((r - 8) & 1) * 1024);
        };
        auto af = [&](int buf, int i) {
          const u32x4 v = {fa[buf][i][0][0], fa[buf][i][0][1], fa[buf][i][1][0], fa[buf][i][1][1]};
          return __builtin_bit_cast(bf16x8, v);
        };
        auto mma = [&](int buf, int m0, int m1, int h = -1) {
#pragma unroll
          for (int m = m0; m < m1; ++m) {
            const int j = m >> 2, i = m & 3;
            if (j < 2) mfma_ip(acc[j][i], fb[buf][j], af(buf, i));
            else mfma_ip(acc[j][i], af(buf, i), fb[buf][j]);
            if (h >= 0 && m - m0 < 14) rd(buf ^ 1, h, m - m0);
          }
        };
        u32x4 one4 = {0x3f803f80u, 0x3f803f80u, 0x3f803f80u, 0x3f803f80u};
        const f32x4 zero4 = {0.f, 0.f, 0.f, 0.f};
        unsigned po = (c >> 2) == g ? rsoff + ct * 1024 + (64 * wr + 32 * wh + c) * 8 : 0xfffffff0u;
        asm volatile("" : "+v"(qa), "+v"(qa2), "+v"(qb), "+v"(one4), "+v"(po));
        int sl = 0;
        auto tile = [&](bool stats) {
          mma(0, 0, 16, 1);
          const int d = sl == 3 ? -3 * QK_STAGE : QK_STAGE;
          sl = (sl + 1) & 3;
          qa += d;
          asm volatile("" : "+v"(qa));
          mma(0, 16, 17);
          qa2 += d;
          asm volatile("" : "+v"(qa2));
          mma(0, 17, 18);
          qb += d;
          asm volatile("" : "+v"(qb));
          mma(0, 18, 23);
          __builtin_amdgcn_s_waitcnt(0xC07F);
          mma(0, 23, 24);
          f32x4 m1[2], m2[2];
          if (stats) {
            if (wh) {
#pragma unroll
              for (int i = 0; i < 2; ++i) {
                m1[i] = MFMA16(__builtin_bit_cast(bf16x8, one4), af(1, 2 + i), MFMA16(__builtin_bit_cast(bf16x8, one4), af(0, 2 + i), zero4));
                m2[i] = MFMA16(af(1, 2 + i), af(1, 2 + i), MFMA16(af(0, 2 + i), af(0, 2 + i), zero4));
              }
            } else {
#pragma unroll
              for (int i = 0; i < 2; ++i) {
                m1[i] = MFMA16(__builtin_bit_cast(bf16x8, one4), af(1, i), MFMA16(__builtin_bit_cast(bf16x8, one4), af(0, i), zero4));
                m2[i] = MFMA16(af(1, i), af(1, i), MFMA16(af(0, i), af(0, i), zero4));
              }
            }
          }
          mma(1, 0, 1);
          lds_barrier();
          mma(1, 1, 17, 0);
          if (stats) {
#pragma unroll
            for (int i = 0; i < 2; ++i) {
              float e0 = m2[i][0], e1 = m2[i][1], e2 = m2[i][2], e3 = m2[i][3], a1 = m1[i][0];
              asm volatile("" : "+v"(e0), "+v"(e1), "+v"(e2), "+v"(e3), "+v"(a1));
              const float a2 = (l & 2) ? ((l & 1) ? e3 : e2) : ((l & 1) ? e1 : e0);
              __builtin_amdgcn_raw_buffer_store_b64((u32x2){__float_as_uint(a1), __float_as_uint(a2) | 1u}, rw, po, i * 128, 16);
              asm volatile("" ::: "memory");
              mma(1, 17 + 3 * i, 20 + 3 * i);
            }
          } else {
            mma(1, 17, 23);
          }
          __builtin_amdgcn_s_waitcnt(0xC07F);
          mma(1, 23, 24);
        };
        ST(2);
        if (layer) {
          __builtin_amdgcn_s_barrier();
          issue_a(0, 0, 0);
        }
        WAIT_VM(0);
        lds_barrier();
        ST(3);
#pragma unroll
        for (int r = 0; r < 14; ++r) rd(0, 0, r);
        __builtin_amdgcn_s_waitcnt(0xC07F);
#pragma unroll 1
        for (int i = 0; i < QK_P; ++i) tile(false);
        tile(true);
#pragma unroll 1
        for (int i = QK_P + 1; i < 15; ++i) tile(false);
        mma(0, 0, 24, 1);
        auto qpart = [&]() {
          const f32x4 cq0 = *(const f32x4*)(s_cs + wh * 32 + 4 * g), cq1 = *(const f32x4*)(s_cs + wh * 32 + 16 + 4 * g);
          const unsigned pqo = WS_PQ + (unsigned)((panel * 32 + 2 * ct + wh) * 8 + 4 * wr) * 1024u + l * 16;
#pragma unroll
          for (int i = 0; i < 4; ++i) {
            const float rs = s_rs[64 * wr + 16 * i + c], nm = s_rs[128 + 64 * wr + 16 * i + c];
            st16<AUX_PQ>(rw, pqo + i * 1024, __builtin_bit_cast(u32x4, pack8(phi4(acc[0][i] * rs + (nm * cq0 + 1e-6f * LOG2E)),
                                                                            phi4(acc[1][i] * rs + (nm * cq1 + 1e-6f * LOG2E)))));
          }
        };
        mma(1, 0, 12);
        lds_barrier();
        mma(1, 12, 24);
        asm volatile("s_nop 7\n\ts_nop 3");
        SB0();
        ST(4);
        qpart();
        SB0();
        {
          f32x4 kv[2][2], zz[2];
          float csk[2], csv[2];
#pragma unroll
          for (int a = 0; a < 2; ++a) {
            kv[a][0] = kv[a][1] = zz[a] = (f32x4){0.f, 0.f, 0.f, 0.f};
            csk[a] = s_cs[64 + wh * 32 + 16 * a + c];
            csv[a] = s_cs[128 + wh * 32 + 16 * a + c];
          }
#pragma unroll
          for (int ip = 0; ip < 2; ++ip) {
            f32x4 rs[2], nm[2];
#pragma unroll
            for (int h = 0; h < 2; ++h) {
              rs[h] = *(const f32x4*)(s_rs + 64 * wr + 32 * ip + 16 * h + 4 * g);
              nm[h] = *(const f32x4*)(s_rs + 128 + 64 * wr + 32 * ip + 16 * h + 4 * g);
            }
            bf16x8 fk[2], fv[2];
#pragma unroll
            for (int a = 0; a < 2; ++a) {
              fk[a] = pack8(phi4(acc[2 + a][2 * ip] * rs[0] + (nm[0] * csk[a] + 1e-6f * LOG2E)),
                            phi4(acc[2 + a][2 * ip + 1] * rs[1] + (nm[1] * csk[a] + 1e-6f * LOG2E)));
            }
#pragma unroll
            for (int h = 0; h < 2; ++h) { rs[h] *= LN2; nm[h] *= LN2; }
#pragma unroll
            for (int a = 0; a < 2; ++a)
              fv[a] = pack8(acc[4 + a][2 * ip] * rs[0] + nm[0] * csv[a], acc[4 + a][2 * ip + 1] * rs[1] + nm[1] * csv[a]);
#pragma unroll
            for (int a = 0; a < 2; ++a) {
              kv[a][0] = MFMA16(fk[a], fv[0], kv[a][0]);
              kv[a][1] = MFMA16(fk[a], fv[1], kv[a][1]);
              zz[a] = MFMA16(fk[a], __builtin_bit_cast(bf16x8, one4), zz[a]);
            }
          }
          float* dst = s_kv + wr * 2112;
#pragma unroll
          for (int qd = 0; qd < 4; ++qd) *(f32x4*)(dst + wh * 1024 + (qd >> 1) * 512 + l * 8 + (qd & 1) * 4) = kv[qd & 1][qd >> 1];
          if (c == 0) { *(f32x4*)(dst + 2048 + wh * 32 + g * 8) = zz[0]; *(f32x4*)(dst + 2048 + wh * 32 + g * 8 + 4) = zz[1]; }
        }
        ST(5);
        WAIT_VM(0);
        lds_barrier();
        ST(6);
        ST(7);
        ST_FLUSH(2 + 2 * layer, ws + WS_DBG);
      }
      {
        PHASE_IDS
        const int rh = w & 1, hp = w >> 1;
        const int row = 32 * rh + 16 * hp + c;
        const unsigned go = (p64 >> 1) * 262144 + (oc * 32 + g) * 1024 + (p64 & 1) * 512 + ((row ^ ((g & 1) * 16)) * 8);
        f32x4 acc[2][8];
#pragma unroll
        for (int s = 0; s < 2; ++s)
#pragma unroll
          for (int n = 0; n < 8; ++n) { acc[s][n] = (f32x4){0.f, 0.f, 0.f, 0.f}; asm volatile("" : "+v"(acc[s][n])); }
        u32x2 sel = {0u, 0u};
        if (g == (c >> 2)) sel[(c >> 1) & 1] = (c & 1) ? 0x3f800000u : 0x3f80u;
        u32x4 sel0 = {sel[0], sel[1], 0u, 0u}, sel1 = {0u, 0u, sel[0], sel[1]};
        const lchar* px = (lchar*)smem + KO_X + g * 512 + ((row ^ ((g & 1) * 16)) * 8);
        asm volatile("" : "+v"(sel0), "+v"(sel1), "+v"(px));
        bf16x8 fq[2], fkv[2], fz, fw[2][8], fo[2];
        f32x4 num[2][2], den[2];
        const f32x4 zero4 = {0.f, 0.f, 0.f, 0.f};
        f32x4 eps4 = {1e-6f, 1e-6f, 1e-6f, 1e-6f};
        asm volatile("" : "+v"(eps4));
        auto M1 = [&](int s) {
          den[s] = MFMA16(fz, fq[s], eps4);
          num[s][0] = MFMA16(fkv[0], fq[s], zero4);
          num[s][1] = MFMA16(fkv[1], fq[s], zero4);
        };
        auto V = [&](int s) {
          const float inv = __builtin_amdgcn_rcpf(den[s][0]);
          f32x4 o0, o1;
#pragma unroll
          for (int e = 0; e < 4; ++e) { o0[e] = mul_nopk(num[s][0][e], inv); o1[e] = mul_nopk(num[s][1][e], inv); }
          fo[s] = pack8(o0, o1);
        };
        auto M2 = [&](int bf, int s) {
#pragma unroll
          for (int n = 0; n < 8; ++n) acc[s][n] = MFMA16(fw[bf][n], fo[s], acc[s][n]);
        };
        const lchar* pq = (lchar*)smem + KO_R + hp * 4096 + rh * 2048 + hp * 1024 + l * 16;
        const lchar* pq2 = pq + (hp ? -1024 : 1024);
        const lchar* pw = (lchar*)smem + KO_R + 8192 + hp * 4096 + l * 16;
        const lchar* pk = (lchar*)smem + KO_R + 24576 + hp * 2048 + l * 16;
        const lchar* pz = (lchar*)smem + KO_Z + hp * 64 + g * 16;
        int sl = 0;
        auto adv = [&]() {
          const int d = sl == 3 ? -3 * KO_STAGE : KO_STAGE;
          sl = (sl + 1) & 3;
          pq += d; pq2 += d; pk += d; pz += 128; pw += d;
          asm volatile("" : "+v"(pq), "+v"(pq2), "+v"(pw), "+v"(pk), "+v"(pz));
        };
        auto rd = [&](int bf) {
          fz = lds_b128(pz); fq[0] = lds_b128(pq); fkv[0] = lds_b128(pk); fkv[1] = lds_b128(pk + 1024); fq[1] = lds_b128(pq2);
#pragma unroll
          for (int n = 0; n < 8; ++n) fw[bf][n] = lds_b128(pw + (n >> 2) * 8192 + (n & 3) * 1024);
        };
        auto period = [&](int bf) {
          rd(bf ^ 1);
          M2(bf, 0); V(1);
#pragma unroll
          for (int i = 0; i < 5; ++i) { SGB(0x8, 1); SGB(0x100, 2); SGB(0x2, 1); }
#pragma unroll
          for (int i = 0; i < 3; ++i) { SGB(0x8, 1); SGB(0x100, 1); SGB(0x2, 3); }
          SB0();
          asm volatile("" :: "v"(den[1]));
          M1(0); M1(1); adv();
          SB0();
          M2(bf, 1);
          V(0);
#pragma unroll
          for (int i = 0; i < 8; ++i) { SGB(0x8, 1); SGB(0x2, 2); }
          SB0();
          asm volatile("" :: "v"(den[0]));
        };
        __builtin_amdgcn_s_barrier();
        ST(2);
        __builtin_amdgcn_s_sleep(G2SLP);
        poll([&]() {
          const u32x2 f = __builtin_amdgcn_raw_buffer_load_b64(rw, opaque(WS_F2 + ((layer * 8 + xcd) * 256 + t) * 16), 0, 16);
          return (f[0] & f[1]) != 0;
        });
        __builtin_amdgcn_s_barrier();
        stage_q(0);
        if (w < 2) blds_n<1, 2>(rw, vk, 65536, lbase + KO_Z + lw * 1024);
        WAIT_VM(0);
        lds_barrier();
        ST(3);
        rd(0);
        adv();
        M1(0); M1(1); V(0);
        SB0();
        for (int u = 0; u < 14; u += 2) {
          lds_barrier();
          SB0();
          period(0);
          lds_barrier();
          SB0();
          period(1);
        }
        lds_barrier();
        SB0();
        period(0);
        lds_barrier();
        SB0();
        bf16x8 fx[4];
#pragma unroll
        for (int k = 0; k < 4; ++k) fx[k] = lds_2b64(px + k * 4096, px + k * 4096 + 2048);
        V(1);
        M2(1, 1);
        SB0();
        {
          char* xb = smem + KO_R + w * 8192 + l * 16;
          M2(1, 0);
#pragma unroll
          for (int n = 0; n < 8; ++n) {
            acc[0][n] = MFMA16(__builtin_bit_cast(bf16x8, (n & 1) ? sel1 : sel0), fx[n >> 1], acc[0][n]);
            *(f32x4*)(xb + n * 1024) = acc[1][n];
          }
#pragma unroll
          for (int n = 0; n < 8; ++n) { SGB(0x8, 2); SGB(0x200, 1); }
        }
        ST(4);
        lds_barrier();
        ST(5);
#pragma unroll
        for (int n = 0; n < 8; ++n) acc[0][n] += *(const f32x4*)(smem + KO_R + (w ^ 2) * 8192 + l * 16 + n * 1024);
        if (layer) {
          char* tb = smem + KO_R + 32768 + w * 4096;
#pragma unroll
          for (int n = 0; n < 8; ++n) *(u32x2*)(tb + c * 256 + (((2 * n + (g >> 1)) ^ c) * 16) + (g & 1) * 8) = (u32x2){pk_bf(acc[0][n][0], acc[0][n][1]), pk_bf(acc[0][n][2], acc[0][n][3])};
          char* o = out + (size_t)(p64 * 64 + 32 * rh + 16 * hp + g) * 2048 + oc * 256 + c * 16;
#pragma unroll
          for (int k = 0; k < 4; ++k) *(u32x4*)(o + k * 8192) = *(const u32x4*)(tb + (4 * k + g) * 256 + ((c ^ (4 * k + g)) * 16));
        } else {
          f32x4 p1 = zero4, p2 = zero4;
#pragma unroll
          for (int n = 0; n < 8; ++n) {
            p1 += acc[0][n];
            p2 += acc[0][n] * acc[0][n];
          }
          const float s1 = swap32_add(swap16_add((p1[0] + p1[1]) + (p1[2] + p1[3])));
          const float s2 = swap32_add(swap16_add((p2[0] + p2[1]) + (p2[2] + p2[3])));
          const unsigned eo = WS_EX + (p64 * (8 * 64) + row) * 8;
          if (l < 16) st8<16>(rw, eo + oc * 512, (u32x2){__float_as_uint(s1), __float_as_uint(s2) | 1u});
          ST(6);
          lds_barrier();
#pragma unroll
          for (int n = 0; n < 8; ++n) {
            const u32x2 y = *(const LAS u32x2*)(px + (KO_Y - KO_X) + n * 2048);
            acc[1][n] = unpack4(y);
            asm volatile("" : "+v"(acc[1][n]));
          }
          u32x2 gv[2];
          __builtin_amdgcn_s_sleep(KO_SLP0);
#pragma unroll 1
          for (unsigned spin = 0; spin < (1u << 17); ++spin) {
            const unsigned po = opaque(eo + 2 * g * 512);
#pragma unroll
            for (int i = 0; i < 2; ++i) gv[i] = __builtin_amdgcn_raw_buffer_load_b64(rw, po + i * 512, 0, 16);
            if (__all(gv[0][1] & gv[1][1] & 1)) break;
            __builtin_amdgcn_s_sleep(KO_SLP);
            asm volatile("" ::: "memory");
          }
          ST(7);
          __builtin_amdgcn_s_barrier();
          const float mean = swap32_add(swap16_add(__uint_as_float(gv[0][0]) + __uint_as_float(gv[1][0]))) * (1.f / D_);
          const float ex2 = swap32_add(swap16_add(__uint_as_float(gv[0][1]) + __uint_as_float(gv[1][1]))) * (1.f / D_);
          const float rs = __builtin_amdgcn_rsqf(fmaxf(ex2 - mean * mean, 0.f) + 1e-5f), nm = -mean * rs;
#pragma unroll
          for (int n = 0; n < 8; ++n) {
            const f32x4 v = acc[0][n] + (acc[0][n] * rs + nm) * acc[1][n];
            st8<AUX_X3>(rw, WS_X3 + go + n * 4096, (u32x2){pk_bf(v[0], v[1]), pk_bf(v[2], v[3])});
          }
          WAIT_VM(0);
          __builtin_amdgcn_raw_buffer_store_b32(1u, rw, l == 0 ? WS_F3 + ((p64 >> 1) * 64 + (p64 & 1) * 32 + oc * 4 + w) * 16 : 0xfffffff0u, 0, 16);
        }
    ST(8);
        ST_FLUSH(3 + 2 * layer, ws + WS_DBG);
      }
    }
  }
}

constexpr int CV_H = 4224, CV_U = 4576, CV_CH = 2 * CV_H + 2 * CV_U;
constexpr int CV_YP = 4096 + 64;
constexpr int CV_Y = 4 * CV_CH;
constexpr int CV_CS = CV_Y + 4 * CV_YP;
constexpr int CV_W = CV_CS + 1024;
constexpr int CV_P = CV_W + 32768;
constexpr int CV_LDS = CV_P + 32768 + 2048;
#define CV_LATE 0
#define CV_WEARLY 0
#define CV_HSLP 50
#define CV_XSLP 20
#define CV_WSLP 0
#define CV_SLP 1
#define CV_SPLIT 8

template <int N0, int N1> DI void conv_tiles(const lchar* aa, const lchar* ba, bf16x8 dfrag, lchar* yb) {
  u32x2 A[2][N1][2];
  bf16x8 B[2][N1];
  f32x4 acc[N1 - N0];
  constexpr int NM = (N1 * (N1 + 1) - N0 * (N0 + 1)) / 2, NR = 3 * N1;
  constexpr int G2 = NR > NM - 4 ? NR - (NM - 4) : 0;
  auto rd = [&](int s, int o, int r) {
    const int p = r / 3, k = r % 3;
    if (k == 0) B[s][N1 - 1 - p] = *(const volatile LAS bf16x8*)(ba + 512 * (8 - N1 + p) + o);
    else A[s][p][k - 1] = *(const volatile LAS u32x2*)(aa + 512 * p + o + 8 * (k - 1));
  };
  auto step = [&](int s, int o, bool more) {
    int m = 0, r = 0;
#pragma unroll
    for (int d = 0; d < N1; ++d)
#pragma unroll
      for (int p = 0; p <= d; ++p) {
        const int dl = N1 - 1 - d, n = p + dl;
        if (n >= N0) {
          const u32x4 af = {A[s][p][0][0], A[s][p][0][1], A[s][p][1][0], A[s][p][1][1]};
          mfma_ip(acc[n - N0], __builtin_bit_cast(bf16x8, af), B[s][dl]);
          if (more && r < NR) rd(s ^ 1, o, r++);
          if (more && m < G2) rd(s ^ 1, o, r++);
          if (more && m == NM - 2) __builtin_amdgcn_s_waitcnt(0xC07F);
          ++m;
        }
      }
  };
#pragma unroll
  for (int r = 0; r < NR; ++r) rd(0, 0, r);
#pragma unroll
  for (int n = N1 - 1; n >= N0; --n) { acc[n - N0] = (f32x4){0.f, 0.f, 0.f, 0.f}; asm volatile("" : "+v"(acc[n - N0])); }
  asm volatile("s_nop 1");
#pragma unroll
  for (int n = N1 - 1; n >= N0; --n) mfma_ip(acc[n - N0], dfrag, B[0][n]);
#pragma unroll 1
  for (int it = 0; it < 3; ++it) {
    step(0, 64, true);
    step(1, 128, true);
    aa += 128; ba += 128;
  }
  step(0, 64, true);
  step(1, 0, false);
  asm volatile("s_nop 7\n\ts_nop 3");
  SB0();
#pragma unroll
  for (int n = N0; n < N1; ++n)
    *(LAS u32x2*)(yb + n * 512) = (u32x2){pk_bf(acc[n - N0][0], acc[n - N0][1]), pk_bf(acc[n - N0][2], acc[n - N0][3])};
}

extern "C" __global__ __launch_bounds__(512, 2) void k_conv(const u16* __restrict__ x0, const u16* __restrict__ kin,
                                                  const u16* __restrict__ vin, const u16* __restrict__ sk,
                                                  const u16* __restrict__ hk, const u16* __restrict__ dvec,
                                                  const u16* __restrict__ wqkv, const u16* __restrict__ wo,
                                                  char* ws) {
  extern __shared__ __attribute__((aligned(16))) char smem[];
  ST_DECL
  asm volatile("" :: "s"(x0), "s"(kin), "s"(vin), "s"(sk), "s"(hk), "s"(dvec), "s"(wqkv), "s"(wo), "s"(ws));
  const int b = blockIdx.x, w = __builtin_amdgcn_readfirstlane(threadIdx.x >> 6), l = threadIdx.x & 63;
  const int xcd = b & 7, slot = b >> 3, t = threadIdx.x & 255;
  const int quad = xcd * 32 + slot, ch = quad * 4 + (w & 3);
  const rsrc_t rw = make_rsrc(ws, WS_END);
  char* cb = smem + (w & 3) * CV_CH;
  const u32x4 z4 = {0, 0, 0, 0};
  auto next_dword = [&](const u32x4 (&c)[4], int i) {
    return (unsigned)__builtin_amdgcn_update_dpp(i < 3 ? __builtin_amdgcn_readfirstlane(c[i < 3 ? i + 1 : 0][0]) : 0, c[i][0], 0x130, 0xf, 0xf, false);
  };
  if (__builtin_expect(w < 4, 1)) {
    const int ctile = 2 * xcd + (slot & 1), rtile = slot >> 1, c8 = l & 7;
    const int ch0 = ctile * 64 + c8 * 8, sl = w * 32 + (l >> 3) * 4, s0 = rtile * 128 + sl;
    const rsrc_t rk = make_rsrc(kin, S_ * D_ * 2), rv = make_rsrc(vin, S_ * D_ * 2);
    char* ps = smem + CV_P;
    u32x4 kr[7], vr[4], wr[4];
    const u32x2 wl = *(const u32x2*)(sk + (ctile * 64 + l) * 4);
#pragma unroll
    for (int i = 0; i < 3; ++i)
      kr[i] = __builtin_amdgcn_raw_buffer_load_b128(rk, l < 8 ? (unsigned)((s0 - 3 + i) * D_ + ch0) * 2u : 0xfffffff0u, 0, NT);
#pragma unroll
    for (int i = 0; i < 4; ++i) kr[3 + i] = __builtin_amdgcn_raw_buffer_load_b128(rk, (unsigned)((s0 + i) * D_ + ch0) * 2u, 0, NT);
#pragma unroll
    for (int i = 0; i < 4; ++i) vr[i] = __builtin_amdgcn_raw_buffer_load_b128(rv, (unsigned)((s0 + i) * D_ + ch0) * 2u, 0, NT);
    unsigned tag = __builtin_amdgcn_raw_buffer_load_b32(rw, WS_NONCE, 0, 16);
    const unsigned dv = (((const unsigned*)dvec)[ch >> 1] >> ((ch & 1) * 16)) & 0xffffu;
    SB0();
    {
      char* ub = cb + 2 * CV_H;
      if (l < 30) { *(u32x4*)(ub + 4096 + l * 16) = z4; *(u32x4*)(ub + CV_U + 4096 + l * 16) = z4; }
    }
    *(u32x2*)(ps + 32768 + w * 512 + l * 8) = wl;
#pragma unroll
    for (int i = 0; i < 4; ++i) wr[i] = *(const u32x4*)(ps + 32768 + w * 512 + c8 * 64 + i * 16);
#pragma unroll
    for (int i = 0; i < 3; ++i)
#pragma unroll
      for (int j = 0; j < 4; ++j) {
        const unsigned x = __builtin_amdgcn_ds_bpermute(4 * l - 32, kr[4 + i][j]);
        kr[i][j] = l < 8 ? kr[i][j] : x;
      }
    char* lp = ps + c8 * 2048 + (((124 - sl) * 2) ^ (c8 * 16));
#pragma unroll
    for (int j = 0; j < 4; ++j) {
      const f32x2 w0 = {bf_lo(wr[j][0]), bf_lo(wr[j][2])}, w1 = {bf_hi(wr[j][0]), bf_hi(wr[j][2])};
      const f32x2 w2 = {bf_lo(wr[j][1]), bf_lo(wr[j][3])}, w3 = {bf_hi(wr[j][1]), bf_hi(wr[j][3])};
      f32x2 kf[7], u[4];
#pragma unroll
      for (int i = 0; i < 7; ++i) kf[i] = (f32x2){bf_lo(kr[i][j]), bf_hi(kr[i][j])};
#pragma unroll
      for (int i = 0; i < 4; ++i)
        u[i] = (kf[i] * w3 + kf[i + 1] * w2 + kf[i + 2] * w1 + kf[i + 3] * w0) * (f32x2){bf_lo(vr[i][j]), bf_hi(vr[i][j])};
#pragma unroll
      for (int c = 0; c < 2; ++c) *(u32x2*)(lp + (2 * j + c) * 256) = (u32x2){pk_bf(u[3][c], u[2][c]), pk_bf(u[1][c], u[0][c])};
    }
    tag += 1;
    asm volatile("" : "+v"(tag));
    ST(2);
    lds_barrier();
#pragma unroll
    for (int i = 0; i < 4; ++i) {
      const int cl = 16 * i + (t >> 4);
      const u32x4 v = *(const u32x4*)(ps + cl * 256 + (((t & 15) ^ (cl >> 3)) * 16));
      st16<16>(rw, WS_UT + (unsigned)((ctile * 64 + cl) * S_ + (S_ - 128 - rtile * 128)) * 2u + (t & 15) * 16, v);
    }
    WAIT_VM(0);
    __builtin_amdgcn_raw_buffer_store_b32(tag, rw, l == 0 ? WS_FU + ((ctile * 4 + w) * 16 + rtile) * 64 : 0xfffffff0u, 0, 16);
    ST(3);
    const int r = l & 15, g = l >> 4;
#pragma unroll 1
    for (unsigned spin = 0; spin < (1u << 17); ++spin) {
      __builtin_amdgcn_s_sleep(CV_SLP);
      const unsigned f = __builtin_amdgcn_raw_buffer_load_b32(rw, opaque(WS_FU + (((quad >> 4) * 4 + (quad & 3)) * 16 + r) * 64), 0, 16);
      asm volatile("" ::: "memory");
      if (__all(f == tag)) break;
    }
    ST(4);
    u32x4 c[4];
#pragma unroll
    for (int i = 0; i < 4; ++i) c[i] = __builtin_amdgcn_raw_buffer_load_b128(rw, WS_UT + ch * (S_ * 2) + l * 16 + i * 1024, 0, 16);
    const lchar* aa = (lchar*)cb + ((r & 1) ? 4 : CV_H) + 4 * r + 16 * g;
    const lchar* ba = (lchar*)cb + 2 * CV_H + ((r >> 3) ? 0 : CV_U) + 448 - 64 * (r & 7) + 16 * g;
    u32x4 dfr;
    const int kk = 30 - 2 * r - 8 * g;
#pragma unroll
    for (int i = 0; i < 4; ++i) dfr[i] = (kk == 2 * i) ? dv : 0u;
    lchar* yb = (lchar*)smem + CV_Y + w * CV_YP + r * 32 + ((g ^ (r >> 2)) & 3) * 8;
    asm volatile("" : "+v"(aa), "+v"(ba), "+v"(dfr), "+v"(yb));
    SB0();
#pragma unroll
    for (int i = 0; i < 4; ++i) {
      char* ub = cb + 2 * CV_H + (l + 64 * i) * 16;
      *(u32x4*)ub = c[i];
      *(u32x4*)(ub + CV_U) =
          (u32x4){__builtin_amdgcn_alignbyte(c[i][1], c[i][0], 2), __builtin_amdgcn_alignbyte(c[i][2], c[i][1], 2),
                  __builtin_amdgcn_alignbyte(c[i][3], c[i][2], 2), __builtin_amdgcn_alignbyte(next_dword(c, i), c[i][3], 2)};
    }
    lds_barrier();
    ST(5);
    conv_tiles<0, 8>(aa, ba, __builtin_bit_cast(bf16x8, dfr), yb);
    ST(6);
    lds_barrier();
    if (slot == 0 && w < 3) CODE_WARM("k_main", 18432, t);
  } else {
    const int hw = w - 4, r16 = quad >> 1, xc0 = (quad & 1) * 512 + hw * 128, c16 = l & 15, g = l >> 4;
    const rsrc_t rx = make_rsrc(x0, S_ * D_ * 2), rh = make_rsrc(hk, S_ * D_ * 2);
    u32x4 xr[4], c[4];
    if (t == 0) *(volatile LAS int*)((lchar*)smem + CV_CS + 512) = 0;
    __builtin_amdgcn_s_sleep(CV_HSLP);
#pragma unroll
    for (int i = 0; i < 4; ++i) c[i] = __builtin_amdgcn_raw_buffer_load_b128(rh, ch * (S_ * 2) + l * 16 + i * 1024, 0, NT);
#pragma unroll
    for (int i = 0; i < 4; ++i) xr[i] = __builtin_amdgcn_raw_buffer_load_b128(rx, (r16 * 16 + 4 * g + i) * (D_ * 2) + xc0 * 2 + c16 * 16, 0, NT);
    unsigned tag = __builtin_amdgcn_raw_buffer_load_b32(rw, WS_NONCE, 0, 16);
    const bool isq = b < 192;
    const int ks = (b & 15) * 2 + (hw & 1), cgp = ((isq ? b : b - 192) >> 4) * 4 + (hw & 2);
    const unsigned pitch = isq ? 6144 : 2048;
    const rsrc_t rwt = make_rsrc(isq ? wqkv : wo, pitch * 1024);
    unsigned vo = (ks * 32 + g) * pitch + cgp * 128 + ((c16 ^ (2 * g)) * 16);
    SB0();
    lds_barrier();
    SB0();
    ST(2);
    if (l < 4) *(u32x4*)(cb + l * 16) = z4;
    if (l < 3) *(u32x4*)(cb + CV_H + l * 16) = z4;
    if (l == 0) *(u32x4*)(cb + CV_H + 48) = (u32x4){0, 0, 0, c[0][0]};
#pragma unroll
    for (int i = 0; i < 4; ++i) {
      const int cc = l + 64 * i;
      *(u32x4*)(cb + 64 + cc * 16) = c[i];
      *(u32x4*)(cb + CV_H + 64 + cc * 16) = (u32x4){c[i][1], c[i][2], c[i][3], next_dword(c, i)};
    }
    SB0();
    __builtin_amdgcn_s_sleep(CV_XSLP);
    {
      char* xs = smem + CV_P + 16384 + hw * 4096;
#pragma unroll
      for (int p = 0; p < 2; ++p)
#pragma unroll
        for (int q = 0; q < 2; ++q)
          *(u32x4*)(xs + (2 * c16 + q) * 128 + (((2 * g + p) ^ (c16 & 7)) * 16)) =
              (u32x4){xr[2 * p][2 * q], xr[2 * p][2 * q + 1], xr[2 * p + 1][2 * q], xr[2 * p + 1][2 * q + 1]};
      const unsigned xo = WS_XQ + (unsigned)((xc0 >> 2) + (l >> 3)) * (S_ * 8) + r16 * 128 + (l & 7) * 16;
#pragma unroll
      for (int i = 0; i < 4; ++i) {
        const int q = 8 * i + (l >> 3);
        st16<16>(rw, xo + i * (8 * S_ * 8), *(const u32x4*)(xs + q * 128 + (((l & 7) ^ ((q >> 1) & 7)) * 16)));
      }
      f32x2* sp = (f32x2*)(smem + CV_CS);
#pragma unroll
      for (int i = 0; i < 4; ++i) {
        f32x2 s1 = {0.f, 0.f}, s2 = s1;
#pragma unroll
        for (int j = 0; j < 4; ++j) {
          const f32x2 v = {bf_lo(xr[i][j]), bf_hi(xr[i][j])};
          s1 += v;
          s2 += v * v;
        }
        float t1 = s1[0] + s1[1], t2 = s2[0] + s2[1];
        t1 += dpp_mov<0xB1>(t1); t2 += dpp_mov<0xB1>(t2);
        t1 += dpp_mov<0x4E>(t1); t2 += dpp_mov<0x4E>(t2);
        t1 += dpp_mov<0x141>(t1); t2 += dpp_mov<0x141>(t2);
        t1 += dpp_mov<0x140>(t1); t2 += dpp_mov<0x140>(t2);
        if (c16 == 0) sp[hw * 16 + 4 * g + i] = (f32x2){t1, t2};
      }
    }
    tag += 1;
    asm volatile("" : "+v"(tag), "+v"(vo));
    {
      asm volatile("s_waitcnt lgkmcnt(0)" ::: "memory");
      int old = 0;
      if (l == 0) old = __hip_atomic_fetch_add((LAS int*)((lchar*)smem + CV_CS + 512), 1, __ATOMIC_RELAXED, __HIP_MEMORY_SCOPE_WORKGROUP);
      if (__builtin_amdgcn_readfirstlane(old) == 3) {
        asm volatile("" ::: "memory");
        const f32x2* sp = (const f32x2*)(smem + CV_CS) + c16;
        const f32x2 v = (sp[0] + sp[16]) + (sp[32] + sp[48]);
        st8<16>(rw, l < 16 ? WS_ST0 + (quad & 1) * 16384 + (r16 * 16 + l) * 8 : 0xfffffff0u, __builtin_bit_cast(u32x2, v));
      }
    }
    WAIT_VM(0);
    __builtin_amdgcn_raw_buffer_store_b32(tag, rw, l == 0 ? WS_FX + (quad * 4 + hw) * 4 : 0xfffffff0u, 0, 16);
    ST(3);
    lds_barrier();
    SB0();
    __builtin_amdgcn_s_sleep(CV_WSLP);
#pragma unroll
    for (int i = 0; i < 8; ++i)
      blds_n<1, 1>(rwt, (i & 1) ? vo ^ 128u : vo, i * 4 * pitch, (unsigned)(size_t)(LAS char*)smem + CV_W + hw * 8192 + i * 1024);

#pragma unroll 1
    for (unsigned spin = 0; spin < (1u << 17); ++spin) {
      const u32x4 f = __builtin_amdgcn_raw_buffer_load_b128(rw, opaque(WS_FX + ((4 * (l >> 4) + hw) * 16 + (l & 15)) * 16), 0, 16);
      asm volatile("" ::: "memory");
      if (__all(f[0] == tag && f[1] == tag && f[2] == tag && f[3] == tag)) break;
      __builtin_amdgcn_s_sleep(2);
    }
    u32x4 xq[4];
    f32x4 st[4], st2[4];
#pragma unroll
    for (int i = 0; i < 4; ++i) {
      st2[i] = __builtin_bit_cast(f32x4, __builtin_amdgcn_raw_buffer_load_b128(rw, WS_ST0 + hw * 1024 + l * 16, 16384 + i * 4096, 16));
      xq[i] = __builtin_amdgcn_raw_buffer_load_b128(rw, WS_XQ + quad * (S_ * 8) + hw * 1024 + l * 16, i * 4096, 16);
      st[i] = __builtin_bit_cast(f32x4, __builtin_amdgcn_raw_buffer_load_b128(rw, WS_ST0 + hw * 1024 + l * 16, i * 4096, 16));
    }
    ST(4);
    SB0();
    bf16x8 fg[8];
    {
      char* wb = smem + CV_W + hw * 8192;
      const int q4 = (l >> 2) & 3;
      const int rd = (4 * g + q4) * 256 + (l & 3) * 8, fr = ((4 * g + q4) & 7) * 32;
#pragma unroll
      for (int i = 0; i < 8; ++i) {
        const char* p = wb + rd + ((i * 32) ^ fr);
        fg[i] = lds_tr2(p, p + 4096);
      }
    }
    {
      const unsigned o = (isq ? WS_WQ + (unsigned)((cgp & 15) * 32 + ks) * 12288u + (cgp >> 4) * 4096u
                              : WS_WO + (unsigned)(cgp * 32 + ks) * 4096u) + l * 16;
      const unsigned o2 = isq ? 32u * 12288u : 32u * 4096u;
#pragma unroll
      for (int i = 0; i < 8; ++i) st16<AUX_W>(rw, o + (i >> 2) * o2 + (i & 3) * 1024, __builtin_bit_cast(u32x4, fg[i]));
      st16<16>(rw, t < 188 ? WS_ZERO + (quad * 188 + t) * 16 : 0xfffffff0u, z4);
#pragma unroll
      for (int i = 0; i < 5; ++i) st16<16>(rw, t + 256 * i < 1056 ? WS_KVP + (b * 1056 + t + 256 * i) * 16 : 0xfffffff0u, z4);
    }
    SB0();
    f32x4 xf[4][2], xn[4][2];
#pragma unroll
    for (int i = 0; i < 4; ++i)
#pragma unroll
      for (int r = 0; r < 2; ++r) {
        xf[i][r] = unpack4((u32x2){xq[i][2 * r], xq[i][2 * r + 1]});
        float mean = st[i][2 * r] + st2[i][2 * r], ex2 = st[i][2 * r + 1] + st2[i][2 * r + 1];
        asm("" : "+v"(mean));
        mean *= 1.f / D_;
        asm("" : "+v"(mean));
        const float rs = __builtin_amdgcn_rsqf(fmaxf(ex2 * (1.f / D_) - mean * mean, 0.f) + 1e-5f), nm = -mean * rs;
#pragma unroll
        for (int e = 0; e < 4; ++e)
          asm("v_fma_f32 %0, %1, %2, %3" : "=v"(xn[i][r][e]) : "v"(xf[i][r][e]), "v"(rs), "v"(nm));
        asm volatile("" : "+v"(xf[i][r]), "+v"(xn[i][r]));
      }
    const int ya = 4 * (hw & 1) + (l >> 4);
    const char* yp = smem + CV_Y + ((l >> 2) & 3) * CV_YP + (hw >> 1) * 512 + ya * 32;
    const int y0 = ((l ^ (ya >> 2)) & 3) * 8, y1 = 256 + ((l ^ (2 + (ya >> 2))) & 3) * 8;
    const unsigned go = (unsigned)hw * 262144 + quad * 1024 + ((l * 16) ^ ((quad & 1) * 128));
    auto fin = [&](int i) {
      const u32x4 y = __builtin_bit_cast(u32x4, lds_tr2(yp + i * 1024 + y0, yp + i * 1024 + y1));
      st16<AUX_X1>(rw, WS_Y + go + i * 1048576, y);
      const f32x4 o0 = xf[i][0] + xn[i][0] * unpack4((u32x2){y[0], y[1]}), o1 = xf[i][1] + xn[i][1] * unpack4((u32x2){y[2], y[3]});
      st16<AUX_X1>(rw, WS_X1 + go + i * 1048576, __builtin_bit_cast(u32x4, pack8(o0, o1)));
    };
    ST(5);
    lds_barrier();
    if (b == 0 && w == 4) __builtin_amdgcn_raw_buffer_store_b32(tag, rw, l == 0 ? WS_NONCE : 0xfffffff0u, 0, 16);
#pragma unroll
    for (int i = 0; i < 4; ++i) fin(i);
    ST(7);
    if (isq) {
      const u32x4 one4 = {0x3f803f80u, 0x3f803f80u, 0x3f803f80u, 0x3f803f80u};
      float* red = (float*)(smem + CV_P);
#pragma unroll
      for (int i = 0; i < 8; ++i) {
        const f32x4 cs = MFMA16(__builtin_bit_cast(bf16x8, one4), fg[i], ((f32x4){0.f, 0.f, 0.f, 0.f}));
        if (l < 16) red[hw * 128 + i * 16 + l] = cs[0];
      }
      lds_barrier();
      const int e = (t >> 7) * 256 + (t & 127);
      ((float*)(ws + WS_CS))[(b & 15) * 3072 + (b >> 4) * 256 + t] = red[e] + red[e + 128];
    }
  }
  ST(8);
  ST_END(1, ws + WS_DBG);
}

extern "C" void kernel_launch(void* const* d_in, const int* in_sizes, int n_in, void* d_out, int out_size, void* d_ws,
                              size_t ws_size, hipStream_t stream) {
  const u16* x0 = (const u16*)d_in[0];
  const u16* kin = (const u16*)d_in[1];
  const u16* vin = (const u16*)d_in[2];
  const u16* sk = (const u16*)d_in[3];
  const u16* hk = (const u16*)d_in[4];
  const u16* dv = (const u16*)d_in[5];
  const u16* wqkv = (const u16*)d_in[7];
  const u16* wo = (const u16*)d_in[8];
  char* ws = (char*)d_ws;
  (void)hipFuncSetAttribute((const void*)k_conv, hipFuncAttributeMaxDynamicSharedMemorySize, CV_LDS);
  (void)hipFuncSetAttribute((const void*)k_main, hipFuncAttributeMaxDynamicSharedMemorySize, QK_LDS);
  k_conv<<<256, 512, CV_LDS, stream>>>(x0, kin, vin, sk, hk, dv, wqkv, wo, ws);
  k_main<<<256, 512, QK_LDS, stream>>>(ws, (char*)d_out);
}
```

```cpp
#include <hip/hip_runtime.h>
#include <stdint.h>

typedef unsigned short u16;
typedef __bf16 bf16x2 __attribute__((ext_vector_type(2)));
typedef __bf16 bf16x4 __attribute__((ext_vector_type(4)));
typedef __bf16 bf16x8 __attribute__((ext_vector_type(8)));
typedef float f32x2 __attribute__((ext_vector_type(2)));
typedef float f32x4 __attribute__((ext_vector_type(4)));
typedef unsigned u32x2 __attribute__((ext_vector_type(2)));
typedef unsigned u32x4 __attribute__((ext_vector_type(4)));
typedef __amdgpu_buffer_rsrc_t rsrc_t;
#define LAS __attribute__((address_space(3)))
#define DI __device__ __forceinline__
typedef LAS char lchar;

#define ST_DECL
#define ST(i)
#define ST_END(k, dbg)
#define ST_FLUSH(k, dbg)
constexpr size_t WS_DBG = 64u << 20;

constexpr int S_ = 2048, D_ = 1024;
constexpr unsigned WS_UT = 0;
constexpr unsigned WS_X1 = 4u << 20;
constexpr unsigned WS_Y = 8u << 20;
constexpr unsigned WS_X3 = 12u << 20;
constexpr unsigned WS_PQ = 16u << 20;
constexpr unsigned WS_WQ = 20u << 20;
constexpr unsigned WS_WO = 26u << 20;
constexpr unsigned WS_XQ = 28u << 20;
constexpr unsigned WS_ST0 = 32u << 20;
constexpr unsigned WS_CS = WS_ST0 + 65536;
constexpr unsigned WS_KVF = WS_CS + 196608;
constexpr unsigned KVF_BYTES = 65536 + 2048;
constexpr unsigned WS_ZERO = WS_KVF + 2 * KVF_BYTES;
constexpr unsigned WS_EX = WS_ZERO;
constexpr unsigned WS_RS = WS_EX + 32 * 8 * 64 * 8;
constexpr unsigned WS_F2 = WS_RS + 2 * 262144;
constexpr unsigned WS_FQ = WS_F2 + 65536;
constexpr unsigned WS_F3 = WS_FQ + 32768;
constexpr unsigned ZERO_BYTES = WS_F3 + 16384 - WS_ZERO;
static_assert(ZERO_BYTES == 256 * 188 * 16, "zeroed region");
constexpr unsigned WS_KVP = 34u << 20;
constexpr unsigned KVP_BLK = 2112 * 4;
constexpr unsigned WS_NONCE = 39u << 20;
constexpr unsigned WS_FU = WS_NONCE + 4096;
constexpr unsigned WS_FX = WS_FU + 65536;
constexpr unsigned WS_F1 = WS_FX + 4096;
constexpr unsigned WS_END = 40u << 20;
constexpr int NT = 2;

#define AUX_UT 16
#define AUX_XQ 0
#define AUX_X1 16
#define AUX_Z 16
#define AUX_W 16
#define AUX_PQ 16
#define AUX_X3 16

extern "C" __device__ float mul_nopk(float, float) __asm("llvm.amdgcn.fmul.legacy");
DI float bf_lo(unsigned u) { return __uint_as_float(u << 16); }
DI float bf_hi(unsigned u) { return __uint_as_float(u & 0xffff0000u); }
DI unsigned pk_bf(float lo, float hi) {
  f32x2 v = {lo, hi};
  return __builtin_bit_cast(unsigned, __builtin_convertvector(v, bf16x2));
}
template <int CTRL> DI float dpp_mov(float x) {
  return __int_as_float(__builtin_amdgcn_update_dpp(0, __float_as_int(x), CTRL, 0xf, 0xf, true));
}
DI float swap32_add(float x) {
  auto r = __builtin_amdgcn_permlane32_swap(__float_as_uint(x), __float_as_uint(x), false, false);
  return __uint_as_float(r[0]) + __uint_as_float(r[1]);
}
DI float swap16_add(float x) {
  auto r = __builtin_amdgcn_permlane16_swap(__float_as_uint(x), __float_as_uint(x), false, false);
  return __uint_as_float(r[0]) + __uint_as_float(r[1]);
}
DI float wave_sum(float x) {
  x += dpp_mov<0xB1>(x);
  x += dpp_mov<0x4E>(x);
  x += dpp_mov<0x141>(x);
  x += dpp_mov<0x140>(x);
  return swap32_add(swap16_add(x));
}
DI void lds_barrier() {
  asm volatile("" ::: "memory");
  __builtin_amdgcn_s_waitcnt(0xC07F);
  __builtin_amdgcn_s_barrier();
  asm volatile("" ::: "memory");
}
#define SB0() __builtin_amdgcn_sched_barrier(0)
#define SGB(mask, n) __builtin_amdgcn_sched_group_barrier(mask, n, 0)
#define WAIT_VM(n) asm volatile("s_waitcnt vmcnt(%0)" :: "n"(n) : "memory")
#define MFMA16(a, b, c) __builtin_amdgcn_mfma_f32_16x16x32_bf16((a), (b), (c), 0, 0, 0)
DI void mfma_ip(f32x4& acc, bf16x8 a, bf16x8 b) { asm volatile("v_mfma_f32_16x16x32_bf16 %0, %1, %2, %0" : "+v"(acc) : "v"(a), "v"(b)); }
DI bf16x8 lds_b128(const char* p) { return *(const bf16x8*)p; }
DI bf16x8 lds_b128(const lchar* p) { return *(const LAS bf16x8*)p; }
DI bf16x8 lds_tr2(const char* p0, const char* p1) {
  bf16x4 a = __builtin_amdgcn_ds_read_tr16_b64_v4bf16((LAS bf16x4*)p0);
  bf16x4 b = __builtin_amdgcn_ds_read_tr16_b64_v4bf16((LAS bf16x4*)p1);
  return __builtin_shufflevector(a, b, 0, 1, 2, 3, 4, 5, 6, 7);
}
DI bf16x8 lds_2b64(const lchar* p0, const lchar* p1) {
  const u32x2 a = *(const volatile LAS u32x2*)p0, b = *(const volatile LAS u32x2*)p1;
  const u32x4 r = {a[0], a[1], b[0], b[1]};
  return __builtin_bit_cast(bf16x8, r);
}
DI f32x4 unpack4(u32x2 v) { return (f32x4){bf_lo(v[0]), bf_hi(v[0]), bf_lo(v[1]), bf_hi(v[1])}; }
DI bf16x8 pack8(f32x4 a, f32x4 b) {
  u32x4 p = {pk_bf(a[0], a[1]), pk_bf(a[2], a[3]), pk_bf(b[0], b[1]), pk_bf(b[2], b[3])};
  return __builtin_bit_cast(bf16x8, p);
}
DI rsrc_t make_rsrc(const void* p, unsigned bytes) { return __builtin_amdgcn_make_buffer_rsrc((void*)p, (short)0, (int)bytes, 0x00020000); }
#define BLDS_(o, p) "buffer_load_dwordx4 %1, %2, %3 offen offset:" #o p " lds\n\t"
#define BLDS_ASM(str) asm volatile("s_mov_b32 m0, %0\n\ts_nop 0\n\t" str :: "s"(lds_base), "v"(voff), "s"(rsrc), "s"(soff) : "memory")
#define BLDS_ALL(p)                                                                       \
  if constexpr (N == 1) BLDS_ASM(BLDS_(0, p));                                            \
  else if constexpr (N == 2) BLDS_ASM(BLDS_(0, p) BLDS_(1024, p));                        \
  else if constexpr (N == 3) BLDS_ASM(BLDS_(0, p) BLDS_(1024, p) BLDS_(2048, p));         \
  else BLDS_ASM(BLDS_(0, p) BLDS_(1024, p) BLDS_(2048, p) BLDS_(3072, p));
template <int N, int P = 0> DI void blds_n(rsrc_t rsrc, unsigned voff, unsigned soff, unsigned lds_base) {
  if constexpr (P == 0) { BLDS_ALL("") } else if constexpr (P == 1) { BLDS_ALL(" nt") } else { BLDS_ALL(" sc1") }
}
#define POL_A 0
#define POL_W 0
constexpr int CODE_BYTES = 30720;
DI void code_warm(int t) {
  unsigned lo, hi;
  asm volatile("s_getpc_b64 s[40:41]\n\ts_add_u32 %0, s40, k_all@rel32@lo+4\n\ts_addc_u32 %1, s41, k_all@rel32@hi+12"
               : "=&s"(lo), "=&s"(hi) :: "scc", "s40", "s41");
  if (t * 128 < CODE_BYTES) {
    const unsigned v = *(const volatile __attribute__((address_space(1))) unsigned*)((((size_t)hi << 32) | lo) + t * 128);
    asm volatile("" :: "v"(v));
  }
}
DI unsigned opaque(unsigned v) { asm volatile("" : "+v"(v) :: "memory"); return v; }
template <int AUX> DI void st8(rsrc_t r, unsigned voff, u32x2 v) { __builtin_amdgcn_raw_buffer_store_b64(v, r, voff, 0, AUX); }
template <int AUX> DI void st16(rsrc_t r, unsigned voff, u32x4 v) { __builtin_amdgcn_raw_buffer_store_b128(v, r, voff, 0, AUX); }

#define QK_SLP0 16
constexpr int QK_STAGE = 40960, QK_LDS = 4 * QK_STAGE;
constexpr int QK_RS = 32768;
constexpr int QK_KV = 57344, QK_KV2 = 150016;
#define QK_P 7
constexpr float LOG2E = 1.44269504088896f, LN2 = 0.693147180559945f;

DI f32x4 phi4(f32x4 u) {
  f32x4 e, m;
#pragma unroll
  for (int i = 0; i < 4; ++i) {
    e[i] = __builtin_amdgcn_fmed3f(__builtin_amdgcn_exp2f(u[i]), 0.f, 1.f);
    m[i] = fmaxf(u[i], 1e-6f * LOG2E);
  }
  return m * LN2 + e;
}

DI float ln_pair(f32x4 p, int g) {
  auto r0 = __builtin_amdgcn_permlane16_swap(__float_as_uint(p[0]), __float_as_uint(p[2]), false, false);
  auto r1 = __builtin_amdgcn_permlane16_swap(__float_as_uint(p[1]), __float_as_uint(p[3]), false, false);
  const float mean = swap32_add(__uint_as_float(r0[0]) + __uint_as_float(r0[1])) * (1.f / D_);
  const float ex2 = swap32_add(__uint_as_float(r1[0]) + __uint_as_float(r1[1])) * (1.f / D_);
  const float rs = __builtin_amdgcn_rsqf(fmaxf(ex2 - mean * mean, 0.f) + 1e-5f) * LOG2E;
  return (g >> 1) ? -mean * rs : rs;
}

#define KO_SLP 1
#define KO_SLP0 4
constexpr int KO_STAGE = 28672;
constexpr int KO_Y = 0, KO_X = 16384;
constexpr int KO_R = 32768;
constexpr int KO_Z = KO_R + 4 * KO_STAGE;
#define G2SLP 8
#define XYPOS 0
#define KVFIRST 1
#define AUX_OUT 16
#define G1SLP 4
#define CW3 1
#define PSLP 1
template <class F> DI void poll(F ok) {
#pragma unroll 1
  for (unsigned spin = 0; spin < (1u << 17); ++spin) {
    const bool r = ok();
    asm volatile("" ::: "memory");
    if (__all(r)) break;
    __builtin_amdgcn_s_sleep(PSLP);
  }
}

#define PHASE_IDS                                                                                                        \
  int b = blockIdx.x, w = w0, l = threadIdx.x & 63;                                                                      \
  unsigned lbase = (unsigned)(size_t)(LAS char*)smem;                                                                    \
  asm volatile("" : "+s"(b), "+s"(w), "+s"(lbase));                                                                      \
  asm volatile("" : "+v"(l));                                                                                             \
  const int xcd = b & 7, slot = b >> 3, lw = w & 3, c = l & 15, g = l >> 4, t = lw * 64 + l;                             \
  const int panel = 8 * (xcd & 1) + (slot & 7), ct = 4 * (xcd >> 1) + (slot >> 3);     \
  const int p64 = 16 * (xcd & 1) + 4 * (xcd & 2) + (slot & 7), oc = 4 * (xcd >> 2) + (slot >> 3);     \
              \
  const unsigned va = (layer ? WS_X3 : WS_X1) + panel * 262144 + lw * 2048 + l * 16;                                     \
  const unsigned vb = WS_WQ + ct * (32 * 12288) + lw * 3072 + l * 16;                                                    \
  bool first = layer == 0;                                                                                                \
  auto kstep = [&](int tt, int h) {                                                                                      \
    const int o = tt < QK_P ? tt : tt - 1, kf = first ? ct : QK_P;            \
    return __builtin_amdgcn_readfirstlane(2 * (tt == QK_P ? kf : (o < kf ? o : o + 1)) + h);                                                           \
  };                                                                                                                     \
  auto issue_a = [&](int tt, int sl, int h) { blds_n<2, 2>(rw, va, kstep(tt, h) * 8192, lbase + sl * QK_STAGE + (4 * h + lw) * 2048); }; \
  auto issue_b = [&](int tt, int sl, int h) { blds_n<3, 2>(rw, vb, kstep(tt, h) * 12288, lbase + sl * QK_STAGE + 16384 + h * 12288 + lw * 3072); }; \
  auto issue = [&](int tt, int sl, int h) { issue_a(tt, sl, h); issue_b(tt, sl, h); };                                   \
  const unsigned vq = WS_PQ + (p64 >> 1) * (32 * 8192) + (lw >> 1) * 8192 + (p64 & 1) * 4096 + (lw & 1) * 2048 + l * 16; \
  const unsigned vw = WS_WO + (2 * oc + (lw >> 1)) * (1024 * 128) + (lw & 1) * 4096 + l * 16;                            \
  const unsigned kvf = WS_KVF + layer * KVF_BYTES, vk = kvf + lw * 1024 + l * 16;                                        \
  auto stage_q = [&](int u) { blds_n<2, 2>(rw, vq, u * 16384, lbase + KO_R + (u & 3) * KO_STAGE + lw * 2048); };         \
  auto stage_w = [&](int u) { blds_n<4, 2>(rw, vw, u * 8192, lbase + KO_R + (u & 3) * KO_STAGE + 8192 + lw * 4096); };      \
  const unsigned rsoff = WS_RS + layer * 262144 + panel * 16384;                                                         \
  (void)c; (void)g; (void)t; (void)issue; (void)stage_q; (void)stage_w; (void)vk; (void)rsoff;

constexpr int CV_H = 4224, CV_U = 4576, CV_CH = 2 * CV_H + 2 * CV_U;
constexpr int CV_YP = 4096 + 64;
constexpr int CV_Y = 4 * CV_CH;
constexpr int CV_CS = CV_Y + 4 * CV_YP;
constexpr int CV_W = CV_CS + 1024;
constexpr int CV_P = CV_W + 32768;
constexpr int CV_LDS = CV_P + 32768 + 2048;
#define CV_LATE 0
#define CV_WEARLY 0
#define CV_HSLP 50
#define CV_XSLP 20
#define CV_WSLP 0
#define CV_SLP 1
#define CV_SPLIT 8

template <int N0, int N1> DI void conv_tiles(const lchar* aa, const lchar* ba, bf16x8 dfrag, lchar* yb) {
  u32x2 A[2][N1][2];
  bf16x8 B[2][N1];
  f32x4 acc[N1 - N0];
  constexpr int NM = (N1 * (N1 + 1) - N0 * (N0 + 1)) / 2, NR = 3 * N1;
  constexpr int G2 = NR > NM - 4 ? NR - (NM - 4) : 0;
  auto rd = [&](int s, int o, int r) {
    const int p = r / 3, k = r % 3;
    if (k == 0) B[s][N1 - 1 - p] = *(const volatile LAS bf16x8*)(ba + 512 * (8 - N1 + p) + o);
    else A[s][p][k - 1] = *(const volatile LAS u32x2*)(aa + 512 * p + o + 8 * (k - 1));
  };
  auto step = [&](int s, int o, bool more) {
    int m = 0, r = 0;
#pragma unroll
    for (int d = 0; d < N1; ++d)
#pragma unroll
      for (int p = 0; p <= d; ++p) {
        const int dl = N1 - 1 - d, n = p + dl;
        if (n >= N0) {
          const u32x4 af = {A[s][p][0][0], A[s][p][0][1], A[s][p][1][0], A[s][p][1][1]};
          mfma_ip(acc[n - N0], __builtin_bit_cast(bf16x8, af), B[s][dl]);
          if (more && r < NR) rd(s ^ 1, o, r++);
          if (more && m < G2) rd(s ^ 1, o, r++);
          if (more && m == NM - 2) __builtin_amdgcn_s_waitcnt(0xC07F);
          ++m;
        }
      }
  };
#pragma unroll
  for (int r = 0; r < NR; ++r) rd(0, 0, r);
#pragma unroll
  for (int n = N1 - 1; n >= N0; --n) { acc[n - N0] = (f32x4){0.f, 0.f, 0.f, 0.f}; asm volatile("" : "+v"(acc[n - N0])); }
  asm volatile("s_nop 1");
#pragma unroll
  for (int n = N1 - 1; n >= N0; --n) mfma_ip(acc[n - N0], dfrag, B[0][n]);
#pragma unroll 1
  for (int it = 0; it < 3; ++it) {
    step(0, 64, true);
    step(1, 128, true);
    aa += 128; ba += 128;
  }
  step(0, 64, true);
  step(1, 0, false);
  asm volatile("s_nop 7\n\ts_nop 3");
  SB0();
#pragma unroll
  for (int n = N0; n < N1; ++n)
    *(LAS u32x2*)(yb + n * 512) = (u32x2){pk_bf(acc[n - N0][0], acc[n - N0][1]), pk_bf(acc[n - N0][2], acc[n - N0][3])};
}

extern "C" __global__ __launch_bounds__(512, 2) void k_all(const u16* __restrict__ x0, const u16* __restrict__ kin,
                                                  const u16* __restrict__ vin, const u16* __restrict__ sk,
                                                  const u16* __restrict__ hk, const u16* __restrict__ dvec,
                                                  const u16* __restrict__ wqkv, const u16* __restrict__ wo,
                                                  char* ws, char* __restrict__ out) {
  extern __shared__ __attribute__((aligned(16))) char smem[];
  ST_DECL
  asm volatile("" :: "s"(x0), "s"(kin), "s"(vin), "s"(sk), "s"(hk), "s"(dvec), "s"(wqkv), "s"(wo), "s"(ws), "s"(out));
  const int w0 = __builtin_amdgcn_readfirstlane(threadIdx.x >> 6);
  const rsrc_t rw = make_rsrc(ws, WS_END);
  float* s_rs = (float*)(smem + QK_RS);
  float* s_cs = s_rs + 256;
  float* s_kv = (float*)(smem + QK_KV);
  if (w0 < 4) {
    unsigned tag1;
   {
    const int b = blockIdx.x, w = w0, l = threadIdx.x & 63;
    const int xcd = b & 7, slot = b >> 3, t = threadIdx.x & 255;
    const int quad = xcd * 32 + slot, ch = quad * 4 + (w & 3);
    char* cb = smem + (w & 3) * CV_CH;
    const u32x4 z4 = {0, 0, 0, 0};
    auto next_dword = [&](const u32x4 (&c)[4], int i) {
      return (unsigned)__builtin_amdgcn_update_dpp(i < 3 ? __builtin_amdgcn_readfirstlane(c[i < 3 ? i + 1 : 0][0]) : 0, c[i][0], 0x130, 0xf, 0xf, false);
    };
    const int ctile = 2 * xcd + (slot & 1), rtile = slot >> 1, c8 = l & 7;
    const int ch0 = ctile * 64 + c8 * 8, sl = w * 32 + (l >> 3) * 4, s0 = rtile * 128 + sl;
    const rsrc_t rk = make_rsrc(kin, S_ * D_ * 2), rv = make_rsrc(vin, S_ * D_ * 2);
    char* ps = smem + CV_P;
    u32x4 kr[7], vr[4], wr[4];
    const u32x2 wl = *(const u32x2*)(sk + (ctile * 64 + l) * 4);
#pragma unroll
    for (int i = 0; i < 3; ++i)
      kr[i] = __builtin_amdgcn_raw_buffer_load_b128(rk, l < 8 ? (unsigned)((s0 - 3 + i) * D_ + ch0) * 2u : 0xfffffff0u, 0, NT);
#pragma unroll
    for (int i = 0; i < 4; ++i) kr[3 + i] = __builtin_amdgcn_raw_buffer_load_b128(rk, (unsigned)((s0 + i) * D_ + ch0) * 2u, 0, NT);
#pragma unroll
    for (int i = 0; i < 4; ++i) vr[i] = __builtin_amdgcn_raw_buffer_load_b128(rv, (unsigned)((s0 + i) * D_ + ch0) * 2u, 0, NT);
    unsigned tag = __builtin_amdgcn_raw_buffer_load_b32(rw, WS_NONCE, 0, 16);
    const unsigned dv = (((const unsigned*)dvec)[ch >> 1] >> ((ch & 1) * 16)) & 0xffffu;
    SB0();
    {
      char* ub = cb + 2 * CV_H;
      if (l < 30) { *(u32x4*)(ub + 4096 + l * 16) = z4; *(u32x4*)(ub + CV_U + 4096 + l * 16) = z4; }
    }
    *(u32x2*)(ps + 32768 + w * 512 + l * 8) = wl;
#pragma unroll
    for (int i = 0; i < 4; ++i) wr[i] = *(const u32x4*)(ps + 32768 + w * 512 + c8 * 64 + i * 16);
#pragma unroll
    for (int i = 0; i < 3; ++i)
#pragma unroll
      for (int j = 0; j < 4; ++j) {
        const unsigned x = __builtin_amdgcn_ds_bpermute(4 * l - 32, kr[4 + i][j]);
        kr[i][j] = l < 8 ? kr[i][j] : x;
      }
    char* lp = ps + c8 * 2048 + (((124 - sl) * 2) ^ (c8 * 16));
#pragma unroll
    for (int j = 0; j < 4; ++j) {
      const f32x2 w0 = {bf_lo(wr[j][0]), bf_lo(wr[j][2])}, w1 = {bf_hi(wr[j][0]), bf_hi(wr[j][2])};
      const f32x2 w2 = {bf_lo(wr[j][1]), bf_lo(wr[j][3])}, w3 = {bf_hi(wr[j][1]), bf_hi(wr[j][3])};
      f32x2 kf[7], u[4];
#pragma unroll
      for (int i = 0; i < 7; ++i) kf[i] = (f32x2){bf_lo(kr[i][j]), bf_hi(kr[i][j])};
#pragma unroll
      for (int i = 0; i < 4; ++i)
        u[i] = (kf[i] * w3 + kf[i + 1] * w2 + kf[i + 2] * w1 + kf[i + 3] * w0) * (f32x2){bf_lo(vr[i][j]), bf_hi(vr[i][j])};
#pragma unroll
      for (int c = 0; c < 2; ++c) *(u32x2*)(lp + (2 * j + c) * 256) = (u32x2){pk_bf(u[3][c], u[2][c]), pk_bf(u[1][c], u[0][c])};
    }
    tag += 1;
    asm volatile("" : "+v"(tag));
    ST(2);
    lds_barrier();
#pragma unroll
    for (int i = 0; i < 4; ++i) {
      const int cl = 16 * i + (t >> 4);
      const u32x4 v = *(const u32x4*)(ps + cl * 256 + (((t & 15) ^ (cl >> 3)) * 16));
      st16<16>(rw, WS_UT + (unsigned)((ctile * 64 + cl) * S_ + (S_ - 128 - rtile * 128)) * 2u + (t & 15) * 16, v);
    }
    WAIT_VM(0);
    __builtin_amdgcn_raw_buffer_store_b32(tag, rw, l == 0 ? WS_FU + ((ctile * 4 + w) * 16 + rtile) * 64 : 0xfffffff0u, 0, 16);
    ST(3);
    const int r = l & 15, g = l >> 4;
#pragma unroll 1
    for (unsigned spin = 0; spin < (1u << 17); ++spin) {
      __builtin_amdgcn_s_sleep(CV_SLP);
      const unsigned f = __builtin_amdgcn_raw_buffer_load_b32(rw, opaque(WS_FU + (((quad >> 4) * 4 + (quad & 3)) * 16 + r) * 64), 0, 16);
      asm volatile("" ::: "memory");
      if (__all(f == tag)) break;
    }
    ST(4);
    u32x4 c[4];
#pragma unroll
    for (int i = 0; i < 4; ++i) c[i] = __builtin_amdgcn_raw_buffer_load_b128(rw, WS_UT + ch * (S_ * 2) + l * 16 + i * 1024, 0, 16);
    const lchar* aa = (lchar*)cb + ((r & 1) ? 4 : CV_H) + 4 * r + 16 * g;
    const lchar* ba = (lchar*)cb + 2 * CV_H + ((r >> 3) ? 0 : CV_U) + 448 - 64 * (r & 7) + 16 * g;
    u32x4 dfr;
    const int kk = 30 - 2 * r - 8 * g;
#pragma unroll
    for (int i = 0; i < 4; ++i) dfr[i] = (kk == 2 * i) ? dv : 0u;
    lchar* yb = (lchar*)smem + CV_Y + w * CV_YP + r * 32 + ((g ^ (r >> 2)) & 3) * 8;
    asm volatile("" : "+v"(aa), "+v"(ba), "+v"(dfr), "+v"(yb));
    SB0();
#pragma unroll
    for (int i = 0; i < 4; ++i) {
      char* ub = cb + 2 * CV_H + (l + 64 * i) * 16;
      *(u32x4*)ub = c[i];
      *(u32x4*)(ub + CV_U) =
          (u32x4){__builtin_amdgcn_alignbyte(c[i][1], c[i][0], 2), __builtin_amdgcn_alignbyte(c[i][2], c[i][1], 2),
                  __builtin_amdgcn_alignbyte(c[i][3], c[i][2], 2), __builtin_amdgcn_alignbyte(next_dword(c, i), c[i][3], 2)};
    }
    lds_barrier();
    ST(5);
    conv_tiles<0, 8>(aa, ba, __builtin_bit_cast(bf16x8, dfr), yb);
    ST(6);
    lds_barrier();
    __builtin_amdgcn_s_barrier();
    __builtin_amdgcn_s_barrier();
    if (slot == 1) code_warm(t);
    ST(8);
    ST_FLUSH(1, ws + WS_DBG);
    tag1 = tag;
   }
    float csum = 0.f;
    u32x2 x3[8];
#pragma unroll
    for (int n = 0; n < 8; ++n) x3[n] = (u32x2){0u, 0u};
#pragma unroll 1
    for (int layer = 0; layer < 2; ++layer) {
      {
        PHASE_IDS
        const int wr = w >> 1, wh = w & 1;
        f32x4 acc[6][4];
#pragma unroll
        for (int j = 0; j < 6; ++j)
#pragma unroll
          for (int i = 0; i < 4; ++i) { acc[j][i] = (f32x4){0.f, 0.f, 0.f, 0.f}; asm volatile("" : "+v"(acc[j][i])); }
        const lchar* qa = (lchar*)smem + g * 1024 + (64 * wr + c) * 8 + (g & 1) * 128;
        const lchar* qa2 = qa + ((g & 1) ? -128 : 128);
        const lchar* qb = (lchar*)smem + 16384 + wh * 2048 + l * 16;
        u32x2 fa[2][4][2];
        bf16x8 fb[2][6];
        auto rd = [&](int buf, int h, int r) {
          if (r == 2) fb[buf][0] = *(const volatile LAS bf16x8*)(qb + h * 12288);
          else if (r < 9) {
            const int k = r < 2 ? r : r - 1, i = k >> 1;
            fa[buf][i][k & 1] = *(const volatile LAS u32x2*)(((i & 1) ? qa2 : qa) + h * 8192 + (i >> 1) * 256 + (k & 1) * 4096);
          } else fb[buf][r - 8] = *(const volatile LAS bf16x8*)(qb + h * 12288 + ((r - 8) >> 1) * 4096 + ((r - 8) & 1) * 1024);
        };
        auto af = [&](int buf, int i) {
          const u32x4 v = {fa[buf][i][0][0], fa[buf][i][0][1], fa[buf][i][1][0], fa[buf][i][1][1]};
          return __builtin_bit_cast(bf16x8, v);
        };
        auto mma = [&](int buf, int m0, int m1, int h = -1) {
#pragma unroll
          for (int m = m0; m < m1; ++m) {
            const int j = m >> 2, i = m & 3;
            if (j < 2) mfma_ip(acc[j][i], fb[buf][j], af(buf, i));
            else mfma_ip(acc[j][i], af(buf, i), fb[buf][j]);
            if (h >= 0 && m - m0 < 14) rd(buf ^ 1, h, m - m0);
          }
        };
        u32x4 one4 = {0x3f803f80u, 0x3f803f80u, 0x3f803f80u, 0x3f803f80u};
        const f32x4 zero4 = {0.f, 0.f, 0.f, 0.f};
        unsigned po = (c >> 2) == g ? rsoff + ct * 1024 + (64 * wr + 32 * wh + c) * 8 : 0xfffffff0u;
        asm volatile("" : "+v"(qa), "+v"(qa2), "+v"(qb), "+v"(one4), "+v"(po));
        int sl = 0;
        auto tile = [&](bool stats) {
          mma(0, 0, 16, 1);
          const int d = sl == 3 ? -3 * QK_STAGE : QK_STAGE;
          sl = (sl + 1) & 3;
          qa += d;
          asm volatile("" : "+v"(qa));
          mma(0, 16, 17);
          qa2 += d;
          asm volatile("" : "+v"(qa2));
          mma(0, 17, 18);
          qb += d;
          asm volatile("" : "+v"(qb));
          mma(0, 18, 23);
          __builtin_amdgcn_s_waitcnt(0xC07F);
          mma(0, 23, 24);
          f32x4 m1[2], m2[2];
          if (stats) {
            if (wh) {
#pragma unroll
              for (int i = 0; i < 2; ++i) {
                m1[i] = MFMA16(__builtin_bit_cast(bf16x8, one4), af(1, 2 + i), MFMA16(__builtin_bit_cast(bf16x8, one4), af(0, 2 + i), zero4));
                m2[i] = MFMA16(af(1, 2 + i), af(1, 2 + i), MFMA16(af(0, 2 + i), af(0, 2 + i), zero4));
              }
            } else {
#pragma unroll
              for (int i = 0; i < 2; ++i) {
                m1[i] = MFMA16(__builtin_bit_cast(bf16x8, one4), af(1, i), MFMA16(__builtin_bit_cast(bf16x8, one4), af(0, i), zero4));
                m2[i] = MFMA16(af(1, i), af(1, i), MFMA16(af(0, i), af(0, i), zero4));
              }
            }
          }
          mma(1, 0, 1);
          lds_barrier();
          mma(1, 1, 17, 0);
          if (stats) {
#pragma unroll
            for (int i = 0; i < 2; ++i) {
              float e0 = m2[i][0], e1 = m2[i][1], e2 = m2[i][2], e3 = m2[i][3], a1 = m1[i][0];
              asm volatile("" : "+v"(e0), "+v"(e1), "+v"(e2), "+v"(e3), "+v"(a1));
              const float a2 = (l & 2) ? ((l & 1) ? e3 : e2) : ((l & 1) ? e1 : e0);
              __builtin_amdgcn_raw_buffer_store_b64((u32x2){__float_as_uint(a1), __float_as_uint(a2) | 1u}, rw, po, i * 128, 16);
              asm volatile("" ::: "memory");
              mma(1, 17 + 3 * i, 20 + 3 * i);
            }
          } else {
            mma(1, 17, 23);
          }
          __builtin_amdgcn_s_waitcnt(0xC07F);
          mma(1, 23, 24);
        };
        float lnv = 0.f;
        ST(2);
        if (layer == 0) {
          __builtin_amdgcn_s_sleep(G1SLP);
          poll([&]() { return __builtin_amdgcn_raw_buffer_load_b32(rw, opaque(WS_F1 + (xcd * 256 + t) * 16), 0, 16) == tag1; });
          __builtin_amdgcn_s_barrier();
          issue(0, 0, 0);
        } else {
          __builtin_amdgcn_s_barrier();
          issue_a(0, 0, 0);
          const unsigned ro = rsoff + 2 * g * 1024 + (32 * w + 2 * c) * 8;
          lnv = ln_pair(__builtin_bit_cast(f32x4, __builtin_amdgcn_raw_buffer_load_b128(rw, ro, 0, 16)) +
                        __builtin_bit_cast(f32x4, __builtin_amdgcn_raw_buffer_load_b128(rw, ro, 1024, 16)), g);
        }
        asm volatile("" : "+v"(lnv));
        WAIT_VM(0);
        lds_barrier();
        ST(3);
#pragma unroll
        for (int r = 0; r < 14; ++r) rd(0, 0, r);
        __builtin_amdgcn_s_waitcnt(0xC07F);
#pragma unroll 1
        for (int i = 0; i < QK_P; ++i) tile(false);
        if (layer == 0) tile(true);
#pragma unroll 1
        for (int i = QK_P + 1 - layer; i < 15; ++i) tile(false);
        mma(0, 0, 24, 1);
        u32x4 pqv[4];
        const unsigned pqo = WS_PQ + (unsigned)((panel * 32 + 2 * ct + wh) * 8 + 4 * wr) * 1024u + l * 16;
        auto qpart = [&]() {
          const f32x4 cq0 = *(const f32x4*)(s_cs + wh * 32 + 4 * g), cq1 = *(const f32x4*)(s_cs + wh * 32 + 16 + 4 * g);
#pragma unroll
          for (int i = 0; i < 4; ++i) {
            const float rs = s_rs[64 * wr + 16 * i + c], nm = s_rs[128 + 64 * wr + 16 * i + c];
            pqv[i] = __builtin_bit_cast(u32x4, pack8(phi4(acc[0][i] * rs + (nm * cq0 + 1e-6f * LOG2E)),
                                                     phi4(acc[1][i] * rs + (nm * cq1 + 1e-6f * LOG2E))));
            if (!KVFIRST) st16<AUX_PQ>(rw, pqo + i * 1024, pqv[i]);
          }
        };
        mma(1, 0, 4);
        if (layer) {
          *(volatile LAS float*)((LAS float*)s_cs + t) = csum;
          *(volatile LAS float*)((LAS float*)s_rs + (g >> 1) * 128 + 32 * w + 2 * c + (g & 1)) = lnv;
        }
        mma(1, 4, 12);
        lds_barrier();
        csum = *(volatile LAS float*)((LAS float*)s_cs + t);
        mma(1, 12, 24);
        asm volatile("s_nop 7\n\ts_nop 3");
        SB0();
        ST(4);
        if (!KVFIRST) qpart();
        SB0();
        {
          f32x4 kv[2][2], zz[2];
          float csk[2], csv[2];
#pragma unroll
          for (int a = 0; a < 2; ++a) {
            kv[a][0] = kv[a][1] = zz[a] = (f32x4){0.f, 0.f, 0.f, 0.f};
            csk[a] = s_cs[64 + wh * 32 + 16 * a + c];
            csv[a] = s_cs[128 + wh * 32 + 16 * a + c];
          }
#pragma unroll
          for (int ip = 0; ip < 2; ++ip) {
            f32x4 rs[2], nm[2];
#pragma unroll
            for (int h = 0; h < 2; ++h) {
              rs[h] = *(const f32x4*)(s_rs + 64 * wr + 32 * ip + 16 * h + 4 * g);
              nm[h] = *(const f32x4*)(s_rs + 128 + 64 * wr + 32 * ip + 16 * h + 4 * g);
            }
            bf16x8 fk[2], fv[2];
#pragma unroll
            for (int a = 0; a < 2; ++a) {
              fk[a] = pack8(phi4(acc[2 + a][2 * ip] * rs[0] + (nm[0] * csk[a] + 1e-6f * LOG2E)),
                            phi4(acc[2 + a][2 * ip + 1] * rs[1] + (nm[1] * csk[a] + 1e-6f * LOG2E)));
            }
#pragma unroll
            for (int h = 0; h < 2; ++h) { rs[h] *= LN2; nm[h] *= LN2; }
#pragma unroll
            for (int a = 0; a < 2; ++a)
              fv[a] = pack8(acc[4 + a][2 * ip] * rs[0] + nm[0] * csv[a], acc[4 + a][2 * ip + 1] * rs[1] + nm[1] * csv[a]);
#pragma unroll
            for (int a = 0; a < 2; ++a) {
              kv[a][0] = MFMA16(fk[a], fv[0], kv[a][0]);
              kv[a][1] = MFMA16(fk[a], fv[1], kv[a][1]);
              zz[a] = MFMA16(fk[a], __builtin_bit_cast(bf16x8, one4), zz[a]);
            }
          }
          float* dst = s_kv + wr * ((QK_KV2 - QK_KV) / 4);
#pragma unroll
          for (int qd = 0; qd < 4; ++qd) *(f32x4*)(dst + wh * 1024 + (qd >> 1) * 512 + l * 8 + (qd & 1) * 4) = kv[qd & 1][qd >> 1];
          if (c == 0) { *(f32x4*)(dst + 2048 + wh * 32 + g * 8) = zz[0]; *(f32x4*)(dst + 2048 + wh * 32 + g * 8 + 4) = zz[1]; }
        }
        ST(5);
        WAIT_VM(0);
        lds_barrier();
        ST(6);
        qpart();
        asm volatile("" : "+v"(pqv[0]), "+v"(pqv[1]), "+v"(pqv[2]), "+v"(pqv[3]));
        __builtin_amdgcn_s_barrier();
#pragma unroll
        for (int i = 0; i < 4; ++i) st16<AUX_PQ>(rw, pqo + i * 1024, pqv[i]);
        __builtin_amdgcn_s_barrier();
        WAIT_VM(0);
        __builtin_amdgcn_raw_buffer_store_b32(1u, rw, l == 0 ? WS_FQ + (((layer * 16 + panel) * 2 + wr) * 32 + ct * 2 + wh) * 16 : 0xfffffff0u, 0, 16);
        ST(7);
        ST_FLUSH(2 + 2 * layer, ws + WS_DBG);
      }
      {
        PHASE_IDS
        const int rh = w & 1, hp = w >> 1;
        const int row = 32 * rh + 16 * hp + c;
        const unsigned go = (p64 >> 1) * 262144 + (oc * 32 + g) * 1024 + (p64 & 1) * 512 + ((row ^ ((g & 1) * 16)) * 8);
        f32x4 acc[2][8];
#pragma unroll
        for (int n = 0; n < 8; ++n) {
          acc[0][n] = unpack4(x3[n]);
          acc[1][n] = (f32x4){0.f, 0.f, 0.f, 0.f};
          asm volatile("" : "+v"(acc[0][n]), "+v"(acc[1][n]));
        }
        u32x2 sel = {0u, 0u};
        if (g == (c >> 2)) sel[(c >> 1) & 1] = (c & 1) ? 0x3f800000u : 0x3f80u;
        u32x4 sel0 = {sel[0], sel[1], 0u, 0u}, sel1 = {0u, 0u, sel[0], sel[1]};
        const lchar* px = (lchar*)smem + KO_X + g * 512 + ((row ^ ((g & 1) * 16)) * 8);
        asm volatile("" : "+v"(sel0), "+v"(sel1), "+v"(px));
        bf16x8 fq[2], fkv[2], fz, fw[2][8], fo[2];
        f32x4 num[2][2], den[2];
        const f32x4 zero4 = {0.f, 0.f, 0.f, 0.f};
        f32x4 eps4 = {1e-6f, 1e-6f, 1e-6f, 1e-6f};
        asm volatile("" : "+v"(eps4));
        auto M1 = [&](int s) {
          den[s] = MFMA16(fz, fq[s], eps4);
          num[s][0] = MFMA16(fkv[0], fq[s], zero4);
          num[s][1] = MFMA16(fkv[1], fq[s], zero4);
        };
        auto V = [&](int s) {
          const float inv = __builtin_amdgcn_rcpf(den[s][0]);
          f32x4 o0, o1;
#pragma unroll
          for (int e = 0; e < 4; ++e) { o0[e] = mul_nopk(num[s][0][e], inv); o1[e] = mul_nopk(num[s][1][e], inv); }
          fo[s] = pack8(o0, o1);
        };
        auto M2 = [&](int bf, int s) {
#pragma unroll
          for (int n = 0; n < 8; ++n) acc[s][n] = MFMA16(fw[bf][n], fo[s], acc[s][n]);
        };
        const lchar* pq = (lchar*)smem + KO_R + hp * 4096 + rh * 2048 + hp * 1024 + l * 16;
        const lchar* pq2 = pq + (hp ? -1024 : 1024);
        const lchar* pw = (lchar*)smem + KO_R + 8192 + hp * 4096 + l * 16;
        const lchar* pk = (lchar*)smem + KO_R + 24576 + hp * 2048 + l * 16;
        const lchar* pz = (lchar*)smem + KO_Z + hp * 64 + g * 16;
        int sl = 0;
        auto adv = [&]() {
          const int d = sl == 3 ? -3 * KO_STAGE : KO_STAGE;
          sl = (sl + 1) & 3;
          pq += d; pq2 += d; pk += d; pz += 128; pw += d;
          asm volatile("" : "+v"(pq), "+v"(pq2), "+v"(pw), "+v"(pk), "+v"(pz));
        };
        auto rd1 = [&]() { fz = lds_b128(pz); fq[0] = lds_b128(pq); fkv[0] = lds_b128(pk); fkv[1] = lds_b128(pk + 1024); fq[1] = lds_b128(pq2); };
        auto rd2 = [&](int bf, int n0, int n1) {
#pragma unroll
          for (int n = n0; n < n1; ++n) fw[bf][n] = lds_b128(pw + (n >> 2) * 8192 + (n & 3) * 1024);
        };
        auto M2p = [&](int bf, int s, int n0, int n1) {
#pragma unroll
          for (int n = n0; n < n1; ++n) acc[s][n] = MFMA16(fw[bf][n], fo[s], acc[s][n]);
        };
        auto period = [&](int bf) {
          rd1();
          M2(bf, 0); V(1);
          SGB(0x100, 1); SGB(0x8, 1); SGB(0x100, 2); SGB(0x8, 1); SGB(0x100, 2);
#pragma unroll
          for (int i = 0; i < 6; ++i) { SGB(0x8, 1); SGB(0x2, 2); }
          SB0();
          asm volatile("" :: "v"(den[1]));
          M2p(bf, 1, 0, 2);
          rd2(bf ^ 1, 0, 2);
          SGB(0x100, 1); SGB(0x8, 1); SGB(0x100, 1); SGB(0x8, 1);
          SB0();
          M1(0); M1(1);
          rd2(bf ^ 1, 2, 8);
#pragma unroll
          for (int i = 0; i < 6; ++i) { SGB(0x8, 1); SGB(0x100, 1); }
          SB0();
          M2p(bf, 1, 2, 8);
          V(0);
          adv();
#pragma unroll
          for (int i = 0; i < 6; ++i) { SGB(0x2, 3); SGB(0x8, 1); }
          SB0();
          asm volatile("" :: "v"(den[0]));
        };
        if (layer == 0) {
#pragma unroll
          for (int k = 0; k < 4; ++k) {
            const bf16x8 fx = lds_2b64(px + k * 4096, px + k * 4096 + 2048);
            acc[0][2 * k] = MFMA16(__builtin_bit_cast(bf16x8, sel0), fx, acc[0][2 * k]);
            acc[0][2 * k + 1] = MFMA16(__builtin_bit_cast(bf16x8, sel1), fx, acc[0][2 * k + 1]);
          }
        }
        ST(2);
        __builtin_amdgcn_s_sleep(G2SLP);
        poll([&]() {
          const u32x2 f = __builtin_amdgcn_raw_buffer_load_b64(rw, opaque(WS_F2 + ((layer * 8 + xcd) * 256 + t) * 16), 0, 16);
          const unsigned fq = __builtin_amdgcn_raw_buffer_load_b32(rw, opaque(WS_FQ + ((layer * 32 + p64) * 32 + w * 8 + (l & 7)) * 16), 0, 16);
          return (f[0] & f[1] & fq) != 0;
        });
        __builtin_amdgcn_s_barrier();
        stage_q(0);
        if (w < 2) blds_n<1, 2>(rw, vk, 65536, lbase + KO_Z + lw * 1024);
        WAIT_VM(0);
        lds_barrier();
        ST(3);
        rd1(); rd2(0, 0, 8);
        adv();
        M1(0); M1(1); V(0);
        SB0();
        for (int u = 0; u < 14; u += 2) {
          lds_barrier();
          SB0();
          period(0);
          lds_barrier();
          SB0();
          period(1);
        }
        lds_barrier();
        SB0();
        period(0);
        V(1);
        M2(1, 1);
        SB0();
        {
          char* xb = smem + KO_R + w * 8192 + l * 16;
          M2(1, 0);
#pragma unroll
          for (int n = 0; n < 8; ++n) *(f32x4*)(xb + n * 1024) = acc[1][n];
#pragma unroll
          for (int n = 0; n < 8; ++n) { SGB(0x8, 1); SGB(0x200, 1); }
        }
        ST(4);
        lds_barrier();
        ST(5);
#pragma unroll
        for (int n = 0; n < 8; ++n) acc[0][n] += *(const f32x4*)(smem + KO_R + (w ^ 2) * 8192 + l * 16 + n * 1024);
        if (layer) {
          char* tb = smem + KO_R + 32768 + w * 4096;
#pragma unroll
          for (int n = 0; n < 8; ++n) *(u32x2*)(tb + c * 256 + (((2 * n + (g >> 1)) ^ c) * 16) + (g & 1) * 8) = (u32x2){pk_bf(acc[0][n][0], acc[0][n][1]), pk_bf(acc[0][n][2], acc[0][n][3])};
          const rsrc_t ro = make_rsrc(out, S_ * D_ * 2);
          const unsigned o = (p64 * 64 + 32 * rh + 16 * hp + g) * 2048 + oc * 256 + c * 16;
#pragma unroll
          for (int k = 0; k < 4; ++k) st16<AUX_OUT>(ro, o + k * 8192, *(const u32x4*)(tb + (4 * k + g) * 256 + ((c ^ (4 * k + g)) * 16)));
        } else {
          f32x4 p1 = zero4, p2 = zero4;
#pragma unroll
          for (int n = 0; n < 8; ++n) {
            p1 += acc[0][n];
            p2 += acc[0][n] * acc[0][n];
          }
          const float s1 = swap32_add(swap16_add((p1[0] + p1[1]) + (p1[2] + p1[3])));
          const float s2 = swap32_add(swap16_add((p2[0] + p2[1]) + (p2[2] + p2[3])));
          const unsigned eo = WS_EX + (p64 * (8 * 64) + row) * 8;
          if (l < 16) st8<16>(rw, eo + oc * 512, (u32x2){__float_as_uint(s1), __float_as_uint(s2) | 1u});
          ST(6);
#pragma unroll
          for (int n = 0; n < 8; ++n) {
            const u32x2 y = *(const LAS u32x2*)(px + (KO_Y - KO_X) + n * 2048);
            acc[1][n] = unpack4(y);
            asm volatile("" : "+v"(acc[1][n]));
          }
          u32x2 gv[2];
          __builtin_amdgcn_s_sleep(KO_SLP0);
#pragma unroll 1
          for (unsigned spin = 0; spin < (1u << 17); ++spin) {
            const unsigned po = opaque(eo + 2 * g * 512);
#pragma unroll
            for (int i = 0; i < 2; ++i) gv[i] = __builtin_amdgcn_raw_buffer_load_b64(rw, po + i * 512, 0, 16);
            if (__all(gv[0][1] & gv[1][1] & 1)) break;
            __builtin_amdgcn_s_sleep(KO_SLP);
            asm volatile("" ::: "memory");
          }
          ST(7);
          const float mean = swap32_add(swap16_add(__uint_as_float(gv[0][0]) + __uint_as_float(gv[1][0]))) * (1.f / D_);
          const float ex2 = swap32_add(swap16_add(__uint_as_float(gv[0][1]) + __uint_as_float(gv[1][1]))) * (1.f / D_);
          const float rs = __builtin_amdgcn_rsqf(fmaxf(ex2 - mean * mean, 0.f) + 1e-5f), nm = -mean * rs;
#pragma unroll
          for (int n = 0; n < 8; ++n) {
            const f32x4 v = acc[0][n] + (acc[0][n] * rs + nm) * acc[1][n];
            x3[n] = (u32x2){pk_bf(v[0], v[1]), pk_bf(v[2], v[3])};
            st8<AUX_X3>(rw, WS_X3 + go + n * 4096, x3[n]);
          }
          SB0();
          p1 = p2 = zero4;
#pragma unroll
          for (int n = 0; n < 8; ++n) {
            const f32x4 v = unpack4(x3[n]);
            p1 += v;
            p2 += v * v;
          }
          const float u1 = swap32_add(swap16_add((p1[0] + p1[1]) + (p1[2] + p1[3])));
          const float u2 = swap32_add(swap16_add((p2[0] + p2[1]) + (p2[2] + p2[3])));
          st8<16>(rw, l < 16 ? WS_RS + 262144 + (p64 >> 1) * 16384 + oc * 1024 + ((p64 & 1) * 64 + row) * 8 : 0xfffffff0u,
                  (u32x2){__float_as_uint(u1), __float_as_uint(u2)});
              __builtin_amdgcn_s_barrier();
          WAIT_VM(0);
          __builtin_amdgcn_raw_buffer_store_b32(1u, rw, l == 0 ? WS_F3 + ((p64 >> 1) * 64 + (p64 & 1) * 32 + oc * 4 + w) * 16 : 0xfffffff0u, 0, 16);
        }
    ST(8);
        ST_FLUSH(3 + 2 * layer, ws + WS_DBG);
      }
    }
    if (blockIdx.x == 0 && threadIdx.x == 0) __builtin_amdgcn_raw_buffer_store_b32(tag1, rw, WS_NONCE, 0, 16);
  } else {
   {
    const int b = blockIdx.x, w = w0, l = threadIdx.x & 63;
    const int xcd = b & 7, slot = b >> 3, t = threadIdx.x & 255;
    const int quad = xcd * 32 + slot, ch = quad * 4 + (w & 3);
    char* cb = smem + (w & 3) * CV_CH;
    const u32x4 z4 = {0, 0, 0, 0};
    auto next_dword = [&](const u32x4 (&c)[4], int i) {
      return (unsigned)__builtin_amdgcn_update_dpp(i < 3 ? __builtin_amdgcn_readfirstlane(c[i < 3 ? i + 1 : 0][0]) : 0, c[i][0], 0x130, 0xf, 0xf, false);
    };
    const int hw = w - 4, r16 = quad >> 1, xc0 = (quad & 1) * 512 + hw * 128, c16 = l & 15, g = l >> 4;
    const rsrc_t rx = make_rsrc(x0, S_ * D_ * 2), rh = make_rsrc(hk, S_ * D_ * 2);
    u32x4 xr[4], c[4];
    if (t == 0) *(volatile LAS int*)((lchar*)smem + CV_CS + 512) = 0;
    if (slot == 0) code_warm(t); else
    __builtin_amdgcn_s_sleep(CV_HSLP);
#pragma unroll
    for (int i = 0; i < 4; ++i) c[i] = __builtin_amdgcn_raw_buffer_load_b128(rh, ch * (S_ * 2) + l * 16 + i * 1024, 0, NT);
#pragma unroll
    for (int i = 0; i < 4; ++i) xr[i] = __builtin_amdgcn_raw_buffer_load_b128(rx, (r16 * 16 + 4 * g + i) * (D_ * 2) + xc0 * 2 + c16 * 16, 0, NT);
    unsigned tag = __builtin_amdgcn_raw_buffer_load_b32(rw, WS_NONCE, 0, 16);
    const bool isq = b < 192;
    const int ks = (b & 15) * 2 + (hw & 1), cgp = ((isq ? b : b - 192) >> 4) * 4 + (hw & 2);
    const unsigned pitch = isq ? 6144 : 2048;
    const rsrc_t rwt = make_rsrc(isq ? wqkv : wo, pitch * 1024);
    unsigned vo = (ks * 32 + g) * pitch + cgp * 128 + ((c16 ^ (2 * g)) * 16);
    SB0();
    lds_barrier();
    SB0();
    ST(2);
    if (l < 4) *(u32x4*)(cb + l * 16) = z4;
    if (l < 3) *(u32x4*)(cb + CV_H + l * 16) = z4;
    if (l == 0) *(u32x4*)(cb + CV_H + 48) = (u32x4){0, 0, 0, c[0][0]};
#pragma unroll
    for (int i = 0; i < 4; ++i) {
      const int cc = l + 64 * i;
      *(u32x4*)(cb + 64 + cc * 16) = c[i];
      *(u32x4*)(cb + CV_H + 64 + cc * 16) = (u32x4){c[i][1], c[i][2], c[i][3], next_dword(c, i)};
    }
    SB0();
    __builtin_amdgcn_s_sleep(CV_XSLP);
    {
      char* xs = smem + CV_P + 16384 + hw * 4096;
#pragma unroll
      for (int p = 0; p < 2; ++p)
#pragma unroll
        for (int q = 0; q < 2; ++q)
          *(u32x4*)(xs + (2 * c16 + q) * 128 + (((2 * g + p) ^ (c16 & 7)) * 16)) =
              (u32x4){xr[2 * p][2 * q], xr[2 * p][2 * q + 1], xr[2 * p + 1][2 * q], xr[2 * p + 1][2 * q + 1]};
      const unsigned xo = WS_XQ + (unsigned)((xc0 >> 2) + (l >> 3)) * (S_ * 8) + r16 * 128 + (l & 7) * 16;
#pragma unroll
      for (int i = 0; i < 4; ++i) {
        const int q = 8 * i + (l >> 3);
        st16<16>(rw, xo + i * (8 * S_ * 8), *(const u32x4*)(xs + q * 128 + (((l & 7) ^ ((q >> 1) & 7)) * 16)));
      }
      f32x2* sp = (f32x2*)(smem + CV_CS);
#pragma unroll
      for (int i = 0; i < 4; ++i) {
        f32x2 s1 = {0.f, 0.f}, s2 = s1;
#pragma unroll
        for (int j = 0; j < 4; ++j) {
          const f32x2 v = {bf_lo(xr[i][j]), bf_hi(xr[i][j])};
          s1 += v;
          s2 += v * v;
        }
        float t1 = s1[0] + s1[1], t2 = s2[0] + s2[1];
        t1 += dpp_mov<0xB1>(t1); t2 += dpp_mov<0xB1>(t2);
        t1 += dpp_mov<0x4E>(t1); t2 += dpp_mov<0x4E>(t2);
        t1 += dpp_mov<0x141>(t1); t2 += dpp_mov<0x141>(t2);
        t1 += dpp_mov<0x140>(t1); t2 += dpp_mov<0x140>(t2);
        if (c16 == 0) sp[hw * 16 + 4 * g + i] = (f32x2){t1, t2};
      }
    }
    tag += 1;
    asm volatile("" : "+v"(tag), "+v"(vo));
    {
      asm volatile("s_waitcnt lgkmcnt(0)" ::: "memory");
      int old = 0;
      if (l == 0) old = __hip_atomic_fetch_add((LAS int*)((lchar*)smem + CV_CS + 512), 1, __ATOMIC_RELAXED, __HIP_MEMORY_SCOPE_WORKGROUP);
      if (__builtin_amdgcn_readfirstlane(old) == 3) {
        asm volatile("" ::: "memory");
        const f32x2* sp = (const f32x2*)(smem + CV_CS) + c16;
        const f32x2 v = (sp[0] + sp[16]) + (sp[32] + sp[48]);
        st8<16>(rw, l < 16 ? WS_ST0 + (quad & 1) * 16384 + (r16 * 16 + l) * 8 : 0xfffffff0u, __builtin_bit_cast(u32x2, v));
      }
    }
    WAIT_VM(0);
    __builtin_amdgcn_raw_buffer_store_b32(tag, rw, l == 0 ? WS_FX + (quad * 4 + hw) * 4 : 0xfffffff0u, 0, 16);
    ST(3);
    lds_barrier();
    SB0();
    __builtin_amdgcn_s_sleep(CV_WSLP);
#pragma unroll
    for (int i = 0; i < 8; ++i)
      blds_n<1, 1>(rwt, (i & 1) ? vo ^ 128u : vo, i * 4 * pitch, (unsigned)(size_t)(LAS char*)smem + CV_W + hw * 8192 + i * 1024);

#pragma unroll 1
    for (unsigned spin = 0; spin < (1u << 17); ++spin) {
      const u32x4 f = __builtin_amdgcn_raw_buffer_load_b128(rw, opaque(WS_FX + ((4 * (l >> 4) + hw) * 16 + (l & 15)) * 16), 0, 16);
      asm volatile("" ::: "memory");
      if (__all(f[0] == tag && f[1] == tag && f[2] == tag && f[3] == tag)) break;
      __builtin_amdgcn_s_sleep(2);
    }
    u32x4 xq[4];
    f32x4 st[4], st2[4];
#pragma unroll
    for (int i = 0; i < 4; ++i) {
      st2[i] = __builtin_bit_cast(f32x4, __builtin_amdgcn_raw_buffer_load_b128(rw, WS_ST0 + hw * 1024 + l * 16, 16384 + i * 4096, 16));
      xq[i] = __builtin_amdgcn_raw_buffer_load_b128(rw, WS_XQ + quad * (S_ * 8) + hw * 1024 + l * 16, i * 4096, 16);
      st[i] = __builtin_bit_cast(f32x4, __builtin_amdgcn_raw_buffer_load_b128(rw, WS_ST0 + hw * 1024 + l * 16, i * 4096, 16));
    }
    ST(4);
    SB0();
    bf16x8 fg[8];
    {
      char* wb = smem + CV_W + hw * 8192;
      const int q4 = (l >> 2) & 3;
      const int rd = (4 * g + q4) * 256 + (l & 3) * 8, fr = ((4 * g + q4) & 7) * 32;
#pragma unroll
      for (int i = 0; i < 8; ++i) {
        const char* p = wb + rd + ((i * 32) ^ fr);
        fg[i] = lds_tr2(p, p + 4096);
      }
    }
    {
      const unsigned o = (isq ? WS_WQ + (unsigned)((cgp & 15) * 32 + ks) * 12288u + (cgp >> 4) * 4096u
                              : WS_WO + (unsigned)(cgp * 32 + ks) * 4096u) + l * 16;
      const unsigned o2 = isq ? 32u * 12288u : 32u * 4096u;
#pragma unroll
      for (int i = 0; i < 8; ++i) st16<AUX_W>(rw, o + (i >> 2) * o2 + (i & 3) * 1024, __builtin_bit_cast(u32x4, fg[i]));
      st16<16>(rw, t < 188 ? WS_ZERO + (quad * 188 + t) * 16 : 0xfffffff0u, z4);
#pragma unroll
      for (int i = 0; i < 5; ++i) st16<16>(rw, t + 256 * i < 1056 ? WS_KVP + (b * 1056 + t + 256 * i) * 16 : 0xfffffff0u, z4);
    }
    SB0();
    f32x4 xf[4][2], xn[4][2];
#pragma unroll
    for (int i = 0; i < 4; ++i)
#pragma unroll
      for (int r = 0; r < 2; ++r) {
        xf[i][r] = unpack4((u32x2){xq[i][2 * r], xq[i][2 * r + 1]});
        float mean = st[i][2 * r] + st2[i][2 * r], ex2 = st[i][2 * r + 1] + st2[i][2 * r + 1];
        asm("" : "+v"(mean));
        mean *= 1.f / D_;
        asm("" : "+v"(mean));
        const float rs = __builtin_amdgcn_rsqf(fmaxf(ex2 * (1.f / D_) - mean * mean, 0.f) + 1e-5f), nm = -mean * rs;
#pragma unroll
        for (int e = 0; e < 4; ++e)
          asm("v_fma_f32 %0, %1, %2, %3" : "=v"(xn[i][r][e]) : "v"(xf[i][r][e]), "v"(rs), "v"(nm));
        asm volatile("" : "+v"(xf[i][r]), "+v"(xn[i][r]));
      }
    const int ya = 4 * (hw & 1) + (l >> 4);
    const char* yp = smem + CV_Y + ((l >> 2) & 3) * CV_YP + (hw >> 1) * 512 + ya * 32;
    const int y0 = ((l ^ (ya >> 2)) & 3) * 8, y1 = 256 + ((l ^ (2 + (ya >> 2))) & 3) * 8;
    const unsigned go = (unsigned)hw * 262144 + quad * 1024 + ((l * 16) ^ ((quad & 1) * 128));
    auto fin = [&](int i) {
      const u32x4 y = __builtin_bit_cast(u32x4, lds_tr2(yp + i * 1024 + y0, yp + i * 1024 + y1));
      st16<AUX_X1>(rw, WS_Y + go + i * 1048576, y);
      const f32x4 o0 = xf[i][0] + xn[i][0] * unpack4((u32x2){y[0], y[1]}), o1 = xf[i][1] + xn[i][1] * unpack4((u32x2){y[2], y[3]});
      st16<AUX_X1>(rw, WS_X1 + go + i * 1048576, __builtin_bit_cast(u32x4, pack8(o0, o1)));
    };
    ST(5);
    lds_barrier();
#pragma unroll
    for (int i = 0; i < 4; ++i) fin(i);
    ST(7);
    {
      const u32x4 one4 = {0x3f803f80u, 0x3f803f80u, 0x3f803f80u, 0x3f803f80u};
      float* red = (float*)(smem + CV_P);
#pragma unroll
      for (int i = 0; i < 8; ++i) {
        const f32x4 cs = MFMA16(__builtin_bit_cast(bf16x8, one4), fg[i], ((f32x4){0.f, 0.f, 0.f, 0.f}));
        if (l < 16) red[hw * 128 + i * 16 + l] = cs[0];
      }
      lds_barrier();
      const int e = (t >> 7) * 256 + (t & 127);
      __builtin_amdgcn_raw_buffer_store_b32(__float_as_uint(red[e] + red[e + 128]), rw,
                                            isq ? WS_CS + ((b & 15) * 3072 + (b >> 4) * 256 + t) * 4 : 0xfffffff0u, 0, 16);
    }
    WAIT_VM(0);
    __builtin_amdgcn_s_barrier();
    if (hw == 0) __builtin_amdgcn_raw_buffer_store_b32(tag, rw, l < 8 ? WS_F1 + (l * 256 + b) * 16 : 0xfffffff0u, 0, 16);
    ST(8);
    ST_FLUSH(1, ws + WS_DBG);
    __builtin_amdgcn_s_barrier();
   }
#pragma unroll 1
    for (int layer = 0; layer < 2; ++layer) {
     {
      PHASE_IDS
      unsigned ro = rsoff + 4 * g * 1024 + (32 * lw + 2 * c) * 8;
      unsigned co = t < 192 ? WS_CS + (4 * g * 3072 + lw * 1024 + ct * 64 + c * 4) * 4 : 0xfffffff0u;
      LAS float* wrs = (LAS float*)s_rs + (g >> 1) * 128 + 32 * lw + 2 * c + (g & 1);
      LAS float* wcs = (LAS float*)s_cs + lw * 64 + c * 4 + g;
      unsigned vx = (p64 >> 1) * 262144 + (oc * 32 + 8 * lw + (l >> 5)) * 1024 + (p64 & 1) * 512 + (l & 31) * 16;
      auto xtile = [&](unsigned off, int o) {
#pragma unroll
        for (int i = 0; i < 4; ++i) blds_n<1, 2>(rw, vx, off + i * 2048, lbase + o + (4 * lw + i) * 1024);
      };
#define LBAR() __builtin_amdgcn_s_barrier()
      if (layer == 0) {
        issue(0, 0, 1); issue(1, 1, 0);
        asm volatile("" : "+v"(ro), "+v"(co), "+v"(wrs), "+v"(wcs), "+v"(vx));
        WAIT_VM(5);
        LBAR();
        issue(1, 1, 1); issue(2, 2, 0); issue(2, 2, 1);
        WAIT_VM(10);
        LBAR();
        issue(3, 3, 0); issue(3, 3, 1); issue(4, 0, 0);
        WAIT_VM(15);
      } else {
        asm volatile("" : "+v"(ro), "+v"(co), "+v"(wrs), "+v"(wcs), "+v"(vx));
        if (lw == 0)
          poll([&]() { return __builtin_amdgcn_raw_buffer_load_b32(rw, opaque(WS_F3 + (panel * 64 + l) * 16), 0, 16) != 0; });
        ST(2);
        __builtin_amdgcn_s_barrier();
        issue_a(0, 0, 1);
#pragma unroll
        for (int i = 2; i < 8; ++i) issue_a(i >> 1, i >> 1, i & 1);
        WAIT_VM(12);
        LBAR();
        WAIT_VM(8);
        LBAR();
        issue(4, 0, 0);
        WAIT_VM(9);
      }
      LBAR();
      for (int u = 2; u < 13; ++u) {
        issue(u + 2, (u + 2) & 3, 1); issue(u + 3, (u + 3) & 3, 0);
        WAIT_VM(15);
        LBAR();
      }
      issue(15, 3, 1);
      if (layer == 0) {
        u32x4 xr[4];
#pragma unroll
        for (int i = 0; i < 4; ++i) xr[i] = __builtin_amdgcn_raw_buffer_load_b128(rw, ro, i * 1024, 16);
        u32x4 cr[4];
#pragma unroll
        for (int i = 0; i < 4; ++i) cr[i] = __builtin_amdgcn_raw_buffer_load_b128(rw, co, i * 12288, 16);
        SB0();
        WAIT_VM(18);
        LBAR();
        SB0();
        __builtin_amdgcn_s_setprio(2);
#pragma unroll 1
        for (unsigned spin = 0; spin < (1u << 17); ++spin) {
          if (__all(xr[0][1] & xr[0][3] & xr[1][1] & xr[1][3] & xr[2][1] & xr[2][3] & xr[3][1] & xr[3][3] & 1)) break;
          __builtin_amdgcn_s_sleep(1);
          ro = opaque(ro);
#pragma unroll
          for (int i = 0; i < 4; ++i) xr[i] = __builtin_amdgcn_raw_buffer_load_b128(rw, ro, i * 1024, 16);
          asm volatile("" ::: "memory");
        }
        __builtin_amdgcn_s_waitcnt(0x0F70);
        SB0();
        f32x4 p;
#pragma unroll
        for (int e = 0; e < 4; ++e) {
          float a = __uint_as_float(xr[0][e]) + __uint_as_float(xr[1][e]), d = __uint_as_float(xr[2][e]) + __uint_as_float(xr[3][e]);
          asm("" : "+v"(a));
          a += d;
          asm volatile("" : "+v"(a));
          p[e] = a;
        }
        SB0();
        LBAR();
        SB0();
        if (XYPOS == 0) { xtile(WS_X1, KO_X); xtile(WS_Y, KO_Y); }
        *wrs = ln_pair(p, g);
        float c0[4];
#pragma unroll
        for (int e = 0; e < 4; ++e) {
          float a = __uint_as_float(cr[0][e]) + __uint_as_float(cr[1][e]), d = __uint_as_float(cr[2][e]) + __uint_as_float(cr[3][e]);
          asm("" : "+v"(a));
          c0[e] = a + d;
          asm("" : "+v"(c0[e]));
        }
        auto q0 = __builtin_amdgcn_permlane16_swap(__float_as_uint(c0[0]), __float_as_uint(c0[1]), false, false);
        auto q1 = __builtin_amdgcn_permlane16_swap(__float_as_uint(c0[2]), __float_as_uint(c0[3]), false, false);
        auto q2 = __builtin_amdgcn_permlane32_swap(__float_as_uint(__uint_as_float(q0[0]) + __uint_as_float(q0[1])),
                                                   __float_as_uint(__uint_as_float(q1[0]) + __uint_as_float(q1[1])), false, false);
        *wcs = __uint_as_float(q2[0]) + __uint_as_float(q2[1]);
        if (XYPOS == 1) { xtile(WS_X1, KO_X); xtile(WS_Y, KO_Y); }
      } else {
        WAIT_VM(10);
        LBAR();
        WAIT_VM(0);
        LBAR();
        __builtin_amdgcn_s_setprio(2);
      }
    ST(3);
      lds_barrier();
      if (layer == 0 && slot == 2) code_warm(t);
      if (XYPOS == 2 && layer == 0) { xtile(WS_X1, KO_X); xtile(WS_Y, KO_Y); }
#pragma unroll
      for (int u = 0; u < 4; ++u) stage_w(u);
      ST(4);
      lds_barrier();
      ST(5);
      const unsigned pb = WS_KVP + (layer * 16 + ct) * (16 * KVP_BLK);
#pragma unroll
      for (int i = 0; i < 3; ++i) {
        const int e = t + 256 * i;
        u32x4 v = __builtin_bit_cast(u32x4, *(const f32x4*)(s_kv + e * 4) + *(const f32x4*)(s_kv + (QK_KV2 - QK_KV) / 4 + e * 4));
        v[1] |= 1u; v[3] |= 1u;
        st16<16>(rw, e < 528 ? pb + panel * KVP_BLK + e * 16 : 0xfffffff0u, v);
      }
      ST(6);
      __builtin_amdgcn_s_sleep(QK_SLP0);
      if (lw == 0) {
        unsigned go = pb + (8 * (l >> 5)) * KVP_BLK + panel * 512 + (l & 31) * 16;
        u32x4 v[8];
#pragma unroll 1
        for (unsigned spin = 0; spin < (1u << 17); ++spin) {
          unsigned ok = 1;
          go = opaque(go);
#pragma unroll
          for (int i = 0; i < 8; ++i) {
            v[i] = __builtin_amdgcn_raw_buffer_load_b128(rw, go, i * KVP_BLK, 16);
            ok &= v[i][1] & v[i][3];
          }
          if (__all(ok)) break;
          asm volatile("" ::: "memory");
        }
        f32x4 a = __builtin_bit_cast(f32x4, v[0]);
#pragma unroll
        for (int i = 1; i < 8; ++i) a += __builtin_bit_cast(f32x4, v[i]);
#pragma unroll
        for (int e = 0; e < 4; ++e) a[e] = swap32_add(a[e]);
        st8<16>(rw, l < 32 ? kvf + ct * 4096 + panel * 256 + l * 8 : 0xfffffff0u, (u32x2){pk_bf(a[0], a[1]), pk_bf(a[2], a[3])});
      } else if (lw == 1 && panel < 8) {
        const unsigned go = pb + ((l >> 1) & 15) * KVP_BLK + 8192 + panel * 32 + (l & 1) * 16;
        u32x4 v;
#pragma unroll 1
        for (unsigned spin = 0; spin < (1u << 17); ++spin) {
          v = __builtin_amdgcn_raw_buffer_load_b128(rw, opaque(go), 0, 16);
          if (__all(v[1] & v[3] & 1)) break;
          asm volatile("" ::: "memory");
        }
        f32x4 a = __builtin_bit_cast(f32x4, v);
#pragma unroll
        for (int e = 0; e < 4; ++e) {
          a[e] += dpp_mov<0x4E>(a[e]);
          a[e] += dpp_mov<0x124>(a[e]);
          a[e] += dpp_mov<0x128>(a[e]);
          a[e] = swap16_add(a[e]);
        }
        st8<16>(rw, l < 2 ? kvf + 65536 + ct * 128 + panel * 16 + l * 8 : 0xfffffff0u, (u32x2){pk_bf(a[0], a[1]), pk_bf(a[2], a[3])});
      }
    ST(7);
      __builtin_amdgcn_s_barrier();
      WAIT_VM(0);
      if (lw < 2)
        __builtin_amdgcn_raw_buffer_store_b32(1u, rw, l < 8 ? WS_F2 + ((layer * 8 + l) * 256 + b) * 16 + lw * 4 : 0xfffffff0u, 0, 16);
      __builtin_amdgcn_s_setprio(0);
      ST_FLUSH(2 + 2 * layer, ws + WS_DBG);
     }
      PHASE_IDS
      auto stage_k = [&](int u) { blds_n<1, 2>(rw, vk, u * 4096, lbase + KO_R + (u & 3) * KO_STAGE + 24576 + lw * 1024); };
      auto stage = [&](int u) { stage_k(u); stage_q(u); stage_w(u); };
      __builtin_amdgcn_s_barrier();
      __builtin_amdgcn_s_barrier();
      ST(2);
      stage_k(0);
#pragma unroll
      for (int u = 1; u < 4; ++u) { stage_k(u); stage_q(u); }
      WAIT_VM(9);
      __builtin_amdgcn_s_barrier();
      WAIT_VM(6);
      __builtin_amdgcn_s_barrier();
      stage(4);
      WAIT_VM(10);
      __builtin_amdgcn_s_barrier();
      for (int u = 2; u < 13; ++u) {
        stage(u + 3);
        WAIT_VM(14);
        __builtin_amdgcn_s_barrier();
      }
      WAIT_VM(7);
      __builtin_amdgcn_s_barrier();
      WAIT_VM(0);
      __builtin_amdgcn_s_barrier();
      __builtin_amdgcn_s_barrier();
      if (layer == 0) {
        __builtin_amdgcn_s_barrier();
        first = false;
#pragma unroll
        for (int i = 0; i < 8; ++i) issue_b(i >> 1, i >> 1, i & 1);
      }
      ST_FLUSH(3 + 2 * layer, ws + WS_DBG);
    }
  }
}

extern "C" void kernel_launch(void* const* d_in, const int* in_sizes, int n_in, void* d_out, int out_size, void* d_ws,
                              size_t ws_size, hipStream_t stream) {
  const u16* x0 = (const u16*)d_in[0];
  const u16* kin = (const u16*)d_in[1];
  const u16* vin = (const u16*)d_in[2];
  const u16* sk = (const u16*)d_in[3];
  const u16* hk = (const u16*)d_in[4];
  const u16* dv = (const u16*)d_in[5];
  const u16* wqkv = (const u16*)d_in[7];
  const u16* wo = (const u16*)d_in[8];
  char* ws = (char*)d_ws;
  (void)hipFuncSetAttribute((const void*)k_all, hipFuncAttributeMaxDynamicSharedMemorySize, QK_LDS);
  k_all<<<256, 512, QK_LDS, stream>>>(x0, kin, vin, sk, hk, dv, wqkv, wo, ws, (char*)d_out);
}
```

```cpp
#include <hip/hip_runtime.h>
#include <stdint.h>

typedef unsigned short u16;
typedef __bf16 bf16x2 __attribute__((ext_vector_type(2)));
typedef __bf16 bf16x4 __attribute__((ext_vector_type(4)));
typedef __bf16 bf16x8 __attribute__((ext_vector_type(8)));
typedef float f32x2 __attribute__((ext_vector_type(2)));
typedef float f32x4 __attribute__((ext_vector_type(4)));
typedef unsigned u32x2 __attribute__((ext_vector_type(2)));
typedef unsigned u32x4 __attribute__((ext_vector_type(4)));
typedef __amdgpu_buffer_rsrc_t rsrc_t;
#define LAS __attribute__((address_space(3)))
#define DI __device__ __forceinline__
typedef LAS char lchar;

constexpr int S_ = 2048, D_ = 1024;
constexpr unsigned WS_UT = 0;
constexpr unsigned WS_X1 = 4u << 20;
constexpr unsigned WS_Y = 8u << 20;
constexpr unsigned WS_X3 = 12u << 20;
constexpr unsigned WS_PQ = 16u << 20;
constexpr unsigned WS_WQ = 20u << 20;
constexpr unsigned WS_WO = 26u << 20;
constexpr unsigned WS_EX0 = 32u << 20;
constexpr unsigned WS_CS = WS_EX0 + 262144;
constexpr unsigned WS_ZERO = WS_CS + 196608;
constexpr unsigned WS_EX = WS_ZERO;
constexpr unsigned WS_RS = WS_EX + 262144;
constexpr unsigned WS_FQ = WS_RS + 2 * 262144;
constexpr unsigned ZERO_BYTES = WS_FQ + 32768 - WS_ZERO;
static_assert(ZERO_BYTES == 256 * 200 * 16, "zeroed region");
constexpr unsigned WS_KVP = 34u << 20;
constexpr unsigned KVP_BLK = 2112 * 2;
constexpr unsigned WS_NONCE = 39u << 20;
constexpr unsigned WS_FU = WS_NONCE + 4096;
constexpr unsigned WS_FX = WS_FU + 65536;
constexpr unsigned WS_F1 = WS_FX + 16384;
constexpr unsigned WS_FY = WS_F1 + 32768;
constexpr unsigned WS_END = 40u << 20;
constexpr int NT = 2;

DI float bf_lo(unsigned u) { return __uint_as_float(u << 16); }
DI float bf_hi(unsigned u) { return __uint_as_float(u & 0xffff0000u); }
DI unsigned pk_bf(float lo, float hi) {
  f32x2 v = {lo, hi};
  return __builtin_bit_cast(unsigned, __builtin_convertvector(v, bf16x2));
}
template <int CTRL> DI float dpp_mov(float x) {
  return __int_as_float(__builtin_amdgcn_update_dpp(0, __float_as_int(x), CTRL, 0xf, 0xf, true));
}
DI float swap32_add(float x) {
  auto r = __builtin_amdgcn_permlane32_swap(__float_as_uint(x), __float_as_uint(x), false, false);
  return __uint_as_float(r[0]) + __uint_as_float(r[1]);
}
DI float swap16_add(float x) {
  auto r = __builtin_amdgcn_permlane16_swap(__float_as_uint(x), __float_as_uint(x), false, false);
  return __uint_as_float(r[0]) + __uint_as_float(r[1]);
}
DI void lds_barrier() {
  asm volatile("" ::: "memory");
  __builtin_amdgcn_s_waitcnt(0xC07F);
  __builtin_amdgcn_s_barrier();
  asm volatile("" ::: "memory");
}
#define SB0() __builtin_amdgcn_sched_barrier(0)
#define WAIT_VM(n) asm volatile("s_waitcnt vmcnt(%0)" :: "n"(n) : "memory")
#define MFMA16(a, b, c) __builtin_amdgcn_mfma_f32_16x16x32_bf16((a), (b), (c), 0, 0, 0)
DI void mfma_ip(f32x4& acc, bf16x8 a, bf16x8 b) { asm volatile("v_mfma_f32_16x16x32_bf16 %0, %1, %2, %0" : "+v"(acc) : "v"(a), "v"(b)); }
#define MFMA_DONE() do { asm volatile("s_nop 7\n\ts_nop 3"); SB0(); } while (0)
DI bf16x8 lds_b128(const char* p) { return *(const bf16x8*)p; }
DI bf16x8 lds_tr2(const char* p0, const char* p1) {
  bf16x4 a = __builtin_amdgcn_ds_read_tr16_b64_v4bf16((LAS bf16x4*)p0);
  bf16x4 b = __builtin_amdgcn_ds_read_tr16_b64_v4bf16((LAS bf16x4*)p1);
  return __builtin_shufflevector(a, b, 0, 1, 2, 3, 4, 5, 6, 7);
}
DI f32x4 unpack4(u32x2 v) { return (f32x4){bf_lo(v[0]), bf_hi(v[0]), bf_lo(v[1]), bf_hi(v[1])}; }
DI bf16x8 pack8(f32x4 a, f32x4 b) {
  u32x4 p = {pk_bf(a[0], a[1]), pk_bf(a[2], a[3]), pk_bf(b[0], b[1]), pk_bf(b[2], b[3])};
  return __builtin_bit_cast(bf16x8, p);
}
DI rsrc_t make_rsrc(const void* p, unsigned bytes) { return __builtin_amdgcn_make_buffer_rsrc((void*)p, (short)0, (int)bytes, 0x00020000); }
#define BLDS_(o, p) "buffer_load_dwordx4 %1, %2, %3 offen offset:" #o p " lds\n\t"
#define BLDS_ASM(str) asm volatile("s_mov_b32 m0, %0\n\ts_nop 0\n\t" str :: "s"(lds_base), "v"(voff), "s"(rsrc), "s"(soff) : "memory")
#define BLDS_ALL(p)                                                                       \
  if constexpr (N == 1) BLDS_ASM(BLDS_(0, p));                                            \
  else if constexpr (N == 2) BLDS_ASM(BLDS_(0, p) BLDS_(1024, p));                        \
  else if constexpr (N == 3) BLDS_ASM(BLDS_(0, p) BLDS_(1024, p) BLDS_(2048, p));         \
  else BLDS_ASM(BLDS_(0, p) BLDS_(1024, p) BLDS_(2048, p) BLDS_(3072, p));
template <int N, int P> DI void blds_n(rsrc_t rsrc, unsigned voff, unsigned soff, unsigned lds_base) {
  if constexpr (P == 1) { BLDS_ALL(" nt") } else { BLDS_ALL(" sc1") }
}
constexpr int CODE_BYTES = 28672;
DI void code_warm(int t) {
  unsigned lo, hi;
  asm volatile("s_getpc_b64 s[40:41]\n\ts_add_u32 %0, s40, k_all@rel32@lo+4\n\ts_addc_u32 %1, s41, k_all@rel32@hi+12"
               : "=&s"(lo), "=&s"(hi) :: "scc", "s40", "s41");
  if (t * 128 < CODE_BYTES) {
    const unsigned v = *(const volatile __attribute__((address_space(1))) unsigned*)((((size_t)hi << 32) | lo) + t * 128);
    asm volatile("" :: "v"(v));
  }
}
DI unsigned opaque(unsigned v) { asm volatile("" : "+v"(v) :: "memory"); return v; }
constexpr int SC1 = 16;
constexpr unsigned NOWHERE = 0xfffffff0u;
DI void st4(rsrc_t r, unsigned voff, unsigned v) { __builtin_amdgcn_raw_buffer_store_b32(v, r, voff, 0, SC1); }
DI void st8(rsrc_t r, unsigned voff, u32x2 v) { __builtin_amdgcn_raw_buffer_store_b64(v, r, voff, 0, SC1); }
DI void st16(rsrc_t r, unsigned voff, u32x4 v) { __builtin_amdgcn_raw_buffer_store_b128(v, r, voff, 0, SC1); }
DI unsigned ld4(rsrc_t r, unsigned voff) { return __builtin_amdgcn_raw_buffer_load_b32(r, voff, 0, SC1); }
DI u32x4 ld16(rsrc_t r, unsigned voff, unsigned soff = 0) { return __builtin_amdgcn_raw_buffer_load_b128(r, voff, soff, SC1); }

constexpr int QK_STAGE = 40960, QK_LDS = 4 * QK_STAGE;
constexpr int QK_RS = 32768;
constexpr int QK_KV = QK_RS + 2048, QK_KV2 = QK_KV + 8448;
constexpr int QK_KF = QK_KV2 + 8448;
constexpr float LOG2E = 1.44269504088896f, LN2 = 0.693147180559945f;

DI f32x4 phi4(f32x4 u) {
  f32x4 e, m;
#pragma unroll
  for (int i = 0; i < 4; ++i) {
    e[i] = __builtin_amdgcn_fmed3f(__builtin_amdgcn_exp2f(u[i]), 0.f, 1.f);
    m[i] = fmaxf(u[i], 1e-6f * LOG2E);
  }
  return m * LN2 + e;
}

DI float ln_pair(f32x4 p, int g) {
  auto r0 = __builtin_amdgcn_permlane16_swap(__float_as_uint(p[0]), __float_as_uint(p[2]), false, false);
  auto r1 = __builtin_amdgcn_permlane16_swap(__float_as_uint(p[1]), __float_as_uint(p[3]), false, false);
  const float mean = swap32_add(__uint_as_float(r0[0]) + __uint_as_float(r0[1])) * (1.f / D_);
  const float ex2 = swap32_add(__uint_as_float(r1[0]) + __uint_as_float(r1[1])) * (1.f / D_);
  const float rs = __builtin_amdgcn_rsqf(fmaxf(ex2 - mean * mean, 0.f) + 1e-5f) * LOG2E;
  return (g >> 1) ? -mean * rs : rs;
}

constexpr int KO_Y = 16384;
constexpr int KO_R = 32768;
template <class F> DI void poll(F ok) {
#pragma unroll 1
  for (unsigned spin = 0; spin < (1u << 17); ++spin) {
    const bool r = ok();
    asm volatile("" ::: "memory");
    if (__all(r)) break;
    __builtin_amdgcn_s_sleep(1);
  }
}

DI u32x4 row_sums(const f32x4 (&x)[8]) {
  u32x4 r;
#pragma unroll
  for (int s = 0; s < 2; ++s) {
    const f32x4 p1 = (x[4 * s] + x[4 * s + 1]) + (x[4 * s + 2] + x[4 * s + 3]);
    const f32x4 p2 = x[4 * s] * x[4 * s] + x[4 * s + 1] * x[4 * s + 1] + (x[4 * s + 2] * x[4 * s + 2] + x[4 * s + 3] * x[4 * s + 3]);
    r[2 * s] = __float_as_uint(swap32_add(swap16_add((p1[0] + p1[1]) + (p1[2] + p1[3]))));
    r[2 * s + 1] = __float_as_uint(swap32_add(swap16_add((p2[0] + p2[1]) + (p2[2] + p2[3])))) | 1u;
  }
  return r;
}
DI u32x2 pack_nz(f32x4 a) {
  const unsigned lo = pk_bf(a[0], a[1]), hi = pk_bf(a[2], a[3]);
  return (u32x2){lo, (lo | hi) ? hi : 0x80000000u};
}
DI void kv_reduce(rsrc_t rw, unsigned pb, int lw, int l, char* smem) {
  const int c = l & 15, g = l >> 4;
  u32x2 sel = {0u, 0u};
  if (g == (c >> 2)) sel[(c >> 1) & 1] = (c & 1) ? 0x3f800000u : 0x3f80u;
  u32x4 sel0 = {sel[0], sel[1], 0u, 0u}, sel1 = {0u, 0u, sel[0], sel[1]};
  asm volatile("" : "+v"(sel0), "+v"(sel1));
  __builtin_amdgcn_s_sleep(16);
  u32x4 v[16];
  u32x2 z;
  f32x4 a, d;
  poll([&]() {
    const unsigned o = opaque(pb + lw * 1024 + l * 16);
#pragma unroll
    for (int i = 0; i < 16; ++i) v[i] = ld16(rw, o, i * KVP_BLK);
    z = __builtin_amdgcn_raw_buffer_load_b64(rw, opaque(pb + (l >> 2) * KVP_BLK + 4096 + lw * 32 + (l & 3) * 8), 0, 16);
    unsigned m = ~0u;
    a = d = (f32x4){0.f, 0.f, 0.f, 0.f};
#pragma unroll
    for (int i = 0; i < 16; ++i) {
      a = MFMA16(__builtin_bit_cast(bf16x8, sel0), __builtin_bit_cast(bf16x8, v[i]), a);
      d = MFMA16(__builtin_bit_cast(bf16x8, sel1), __builtin_bit_cast(bf16x8, v[i]), d);
      m = min(m, min(v[i][0] | v[i][1], v[i][2] | v[i][3]));
    }
    return m != 0 && (z[0] | z[1]) != 0;
  });
  *(bf16x8*)(smem + QK_KF + lw * 1024 + l * 16) = pack8(a, d);
  f32x4 zf = unpack4(z);
#pragma unroll
  for (int e = 0; e < 4; ++e) {
    zf[e] += dpp_mov<0x124>(zf[e]);
    zf[e] += dpp_mov<0x128>(zf[e]);
    zf[e] = swap32_add(swap16_add(zf[e]));
  }
  if (l < 4) *(u32x2*)(smem + QK_KF + 4096 + lw * 32 + l * 8) = (u32x2){pk_bf(zf[0], zf[1]), pk_bf(zf[2], zf[3])};
}

#define PHASE_IDS                                                                                                        \
  int b = blockIdx.x, w = w0, l = threadIdx.x & 63;                                                                      \
  unsigned lbase = (unsigned)(size_t)(LAS char*)smem;                                                                    \
  asm volatile("" : "+s"(b), "+s"(w), "+s"(lbase));                                                                      \
  asm volatile("" : "+v"(l));                                                                                             \
  const int xcd = b & 7, slot = b >> 3, lw = w & 3, c = l & 15, g = l >> 4, t = lw * 64 + l;                             \
  const int panel = 8 * (xcd & 1) + (slot & 7), ct = 4 * (xcd >> 1) + (slot >> 3);     \
                 \
  const unsigned va = (layer ? WS_X3 : WS_X1) + panel * 262144 + lw * 2048 + l * 16;                                     \
  const unsigned vb = WS_WQ + ct * (32 * 12288) + lw * 3072 + l * 16;                                                    \
  auto kstep = [&](int tt, int h) { return __builtin_amdgcn_readfirstlane(2 * (tt == 0 ? ct : (tt <= ct ? tt - 1 : tt)) + h); };   \
  auto issue_a = [&](int tt, int sl, int h) { blds_n<2, 2>(rw, va, kstep(tt, h) * 8192, lbase + sl * QK_STAGE + (4 * h + lw) * 2048); }; \
  auto issue_b = [&](int tt, int sl, int h) { blds_n<3, 2>(rw, vb, kstep(tt, h) * 12288, lbase + sl * QK_STAGE + 16384 + h * 12288 + lw * 3072); }; \
  auto issue = [&](int tt, int sl, int h) { issue_a(tt, sl, h); issue_b(tt, sl, h); };                                   \
  const unsigned vw = WS_WO + ct * 131072 + lw * 4096 + l * 16;                                                          \
  auto stage_w = [&](int u) { blds_n<4, 2>(rw, vw, u * 16384, lbase + KO_R + ((u + 2) & 7) * 16384 + lw * 4096); };         \
  const unsigned pb = WS_KVP + (layer * 16 + ct) * (16 * KVP_BLK);                                                       \
  const unsigned rsoff = WS_RS + layer * 262144 + panel * 16384;                                                         \
  (void)c; (void)g; (void)t; (void)issue; (void)stage_w; (void)pb; (void)rsoff;

constexpr int CV_H = 4224, CV_U = 4576, CV_CH = 2 * CV_H + 2 * CV_U;
constexpr int CV_YP = 4096 + 64;
constexpr int CV_Y = 4 * CV_CH;
constexpr int CV_W = CV_Y + 4 * CV_YP + 1024;
constexpr int CV_P = CV_W + 32768;
static_assert(CV_P + 32768 + 2048 <= QK_LDS, "LDS");

template <class F> DI void conv_tiles(const lchar* aa, const lchar* ba, bf16x8 dfrag, lchar* yb, F fill) {
  u32x2 A[2][8][2];
  bf16x8 B[2][8];
  f32x4 acc[8];
  auto rd = [&](int s, int o, int r) {
    const int p = r / 3, k = r % 3;
    if (k == 0) B[s][7 - p] = *(const volatile LAS bf16x8*)(ba + 512 * p + o);
    else A[s][p][k - 1] = *(const volatile LAS u32x2*)(aa + 512 * p + o + 8 * (k - 1));
  };
  auto step = [&](int s, int o, bool more) {
    int m = 0;
#pragma unroll
    for (int d = 0; d < 8; ++d)
#pragma unroll
      for (int p = 0; p <= d; ++p) {
        const u32x4 af = {A[s][p][0][0], A[s][p][0][1], A[s][p][1][0], A[s][p][1][1]};
        mfma_ip(acc[p + 7 - d], __builtin_bit_cast(bf16x8, af), B[s][7 - d]);
        if (more && m < 24) rd(s ^ 1, o, m);
        if (more && m == 34) __builtin_amdgcn_s_waitcnt(0xC07F);
        ++m;
      }
  };
#pragma unroll
  for (int n = 7; n >= 0; --n) { acc[n] = (f32x4){0.f, 0.f, 0.f, 0.f}; asm volatile("" : "+v"(acc[n])); }
  SB0();
  fill();
#pragma unroll
  for (int r = 0; r < 24; r += 3) rd(0, 0, r);
#pragma unroll
  for (int r = 0; r < 24; ++r) if (r % 3) rd(0, 0, r);
#pragma unroll
  for (int n = 7; n >= 0; --n) mfma_ip(acc[n], dfrag, B[0][n]);
#pragma unroll 1
  for (int it = 0; it < 3; ++it) {
    step(0, 64, true);
    step(1, 128, true);
    aa += 128; ba += 128;
  }
  step(0, 64, true);
  step(1, 0, false);
  MFMA_DONE();
#pragma unroll
  for (int n = 0; n < 8; ++n) *(LAS u32x2*)(yb + n * 512) = (u32x2){pk_bf(acc[n][0], acc[n][1]), pk_bf(acc[n][2], acc[n][3])};
}
DI unsigned next_dword(const u32x4 (&c)[4], int i) {
  return (unsigned)__builtin_amdgcn_update_dpp(i < 3 ? __builtin_amdgcn_readfirstlane(c[i < 3 ? i + 1 : 0][0]) : 0, c[i][0], 0x130, 0xf, 0xf, false);
}
DI unsigned next_tag(unsigned t) { return t * 1664525u + 1013904223u; }

extern "C" __global__ __launch_bounds__(512, 2) void k_all(const u16* __restrict__ x0, const u16* __restrict__ kin,
                                                  const u16* __restrict__ vin, const u16* __restrict__ sk,
                                                  const u16* __restrict__ hk, const u16* __restrict__ dvec,
                                                  const u16* __restrict__ wqkv, const u16* __restrict__ wo,
                                                  char* ws, char* __restrict__ out) {
  extern __shared__ __attribute__((aligned(16))) char smem[];
  asm volatile("" :: "s"(x0), "s"(kin), "s"(vin), "s"(sk), "s"(hk), "s"(dvec), "s"(wqkv), "s"(wo), "s"(ws), "s"(out));
  const int w0 = __builtin_amdgcn_readfirstlane(threadIdx.x >> 6);
  const rsrc_t rw = make_rsrc(ws, WS_END);
  float* s_rs = (float*)(smem + QK_RS);
  float* s_cs = s_rs + 256;
  float* s_kv = (float*)(smem + QK_KV);
  if (w0 < 4) {
    unsigned tag1;
    f32x4 xa[8];
   {
    const int b = blockIdx.x, w = w0, l = threadIdx.x & 63;
    const int xcd = b & 7, slot = b >> 3, t = threadIdx.x & 255;
    const int quad = xcd * 32 + slot, ch = quad * 4 + (w & 3);
    char* cb = smem + (w & 3) * CV_CH;
    const u32x4 z4 = {0, 0, 0, 0};
    const int ctile = 2 * xcd + (slot & 1), rtile = slot >> 1, c8 = l & 7;
    const int ch0 = ctile * 64 + c8 * 8, sl = w * 32 + (l >> 3) * 4, s0 = rtile * 128 + sl;
    const rsrc_t rk = make_rsrc(kin, S_ * D_ * 2), rv = make_rsrc(vin, S_ * D_ * 2);
    char* ps = smem + CV_P;
    u32x4 kr[7], vr[4], wr[4];
    const u32x2 wl = *(const u32x2*)(sk + (ctile * 64 + l) * 4);
#pragma unroll
    for (int i = 0; i < 3; ++i)
      kr[i] = __builtin_amdgcn_raw_buffer_load_b128(rk, l < 8 ? (unsigned)((s0 - 3 + i) * D_ + ch0) * 2u : NOWHERE, 0, NT);
#pragma unroll
    for (int i = 0; i < 4; ++i) kr[3 + i] = __builtin_amdgcn_raw_buffer_load_b128(rk, (unsigned)((s0 + i) * D_ + ch0) * 2u, 0, NT);
#pragma unroll
    for (int i = 0; i < 4; ++i) vr[i] = __builtin_amdgcn_raw_buffer_load_b128(rv, (unsigned)((s0 + i) * D_ + ch0) * 2u, 0, NT);
    unsigned tag = ld4(rw, WS_NONCE);
    const unsigned dv = (((const unsigned*)dvec)[ch >> 1] >> ((ch & 1) * 16)) & 0xffffu;
    SB0();
    {
      char* ub = cb + 2 * CV_H;
      if (l < 30) { *(u32x4*)(ub + 4096 + l * 16) = z4; *(u32x4*)(ub + CV_U + 4096 + l * 16) = z4; }
    }
    *(u32x2*)(ps + 32768 + w * 512 + l * 8) = wl;
#pragma unroll
    for (int i = 0; i < 4; ++i) wr[i] = *(const u32x4*)(ps + 32768 + w * 512 + c8 * 64 + i * 16);
#pragma unroll
    for (int i = 0; i < 3; ++i)
#pragma unroll
      for (int j = 0; j < 4; ++j) {
        const unsigned x = __builtin_amdgcn_ds_bpermute(4 * l - 32, kr[4 + i][j]);
        kr[i][j] = l < 8 ? kr[i][j] : x;
      }
    char* lp = ps + c8 * 2048 + (((124 - sl) * 2) ^ (c8 * 16));
#pragma unroll
    for (int j = 0; j < 4; ++j) {
      const f32x2 w0 = {bf_lo(wr[j][0]), bf_lo(wr[j][2])}, w1 = {bf_hi(wr[j][0]), bf_hi(wr[j][2])};
      const f32x2 w2 = {bf_lo(wr[j][1]), bf_lo(wr[j][3])}, w3 = {bf_hi(wr[j][1]), bf_hi(wr[j][3])};
      f32x2 kf[7], u[4];
#pragma unroll
      for (int i = 0; i < 7; ++i) kf[i] = (f32x2){bf_lo(kr[i][j]), bf_hi(kr[i][j])};
#pragma unroll
      for (int i = 0; i < 4; ++i)
        u[i] = (kf[i] * w3 + kf[i + 1] * w2 + kf[i + 2] * w1 + kf[i + 3] * w0) * (f32x2){bf_lo(vr[i][j]), bf_hi(vr[i][j])};
#pragma unroll
      for (int c = 0; c < 2; ++c) *(u32x2*)(lp + (2 * j + c) * 256) = (u32x2){pk_bf(u[3][c], u[2][c]), pk_bf(u[1][c], u[0][c])};
    }
    tag = next_tag(tag);
    asm volatile("" : "+v"(tag));
    lds_barrier();
#pragma unroll
    for (int i = 0; i < 4; ++i) {
      const int cl = 16 * i + (t >> 4);
      const u32x4 v = *(const u32x4*)(ps + cl * 256 + (((t & 15) ^ (cl >> 3)) * 16));
      st16(rw, WS_UT + (unsigned)((ctile * 64 + cl) * S_ + (S_ - 128 - rtile * 128)) * 2u + (t & 15) * 16, v);
    }
    WAIT_VM(0);
    st4(rw, l == 0 ? WS_FU + ((ctile * 4 + w) * 16 + rtile) * 64 : NOWHERE, tag);
    const int r = l & 15, g = l >> 4;
    poll([&]() { return ld4(rw, opaque(WS_FU + (((quad >> 4) * 4 + (quad & 3)) * 16 + r) * 64)) == tag; });
    u32x4 c[4];
#pragma unroll
    for (int i = 0; i < 4; ++i) c[i] = ld16(rw, WS_UT + ch * (S_ * 2) + l * 16 + i * 1024);
    const lchar* aa = (lchar*)cb + ((r & 1) ? 4 : CV_H) + 4 * r + 16 * g;
    const lchar* ba = (lchar*)cb + 2 * CV_H + ((r >> 3) ? 0 : CV_U) + 448 - 64 * (r & 7) + 16 * g;
    u32x4 dfr;
    const int kk = 30 - 2 * r - 8 * g;
#pragma unroll
    for (int i = 0; i < 4; ++i) dfr[i] = (kk == 2 * i) ? dv : 0u;
    lchar* yb = (lchar*)smem + CV_Y + w * CV_YP + r * 32 + ((g ^ (r >> 2)) & 3) * 8;
    asm volatile("" : "+v"(aa), "+v"(ba), "+v"(dfr), "+v"(yb));
    SB0();
    conv_tiles(aa, ba, __builtin_bit_cast(bf16x8, dfr), yb, [&]() {
#pragma unroll
      for (int i = 0; i < 4; ++i) {
        char* ub = cb + 2 * CV_H + (l + 64 * i) * 16;
        *(u32x4*)ub = c[i];
        *(u32x4*)(ub + CV_U) =
            (u32x4){__builtin_amdgcn_alignbyte(c[i][1], c[i][0], 2), __builtin_amdgcn_alignbyte(c[i][2], c[i][1], 2),
                    __builtin_amdgcn_alignbyte(c[i][3], c[i][2], 2), __builtin_amdgcn_alignbyte(next_dword(c, i), c[i][3], 2)};
      }
      lds_barrier();
    });
    lds_barrier();
    {
      const int q4 = (l >> 2) & 3, ya = 4 * (w & 1) + g;
      const char* yp = smem + CV_Y + q4 * CV_YP + (w >> 1) * 512 + ya * 32;
      const int y0 = ((l ^ (ya >> 2)) & 3) * 8, y1 = 256 + ((l ^ (2 + (ya >> 2))) & 3) * 8;
      const unsigned go = WS_Y + (unsigned)w * 262144 + quad * 1024 + ((l * 16) ^ ((quad & 1) * 128));
#pragma unroll
      for (int i = 0; i < 4; ++i) st16(rw, go + i * 1048576, __builtin_bit_cast(u32x4, lds_tr2(yp + i * 1024 + y0, yp + i * 1024 + y1)));
      const u32x4 one4 = {0x3f803f80u, 0x3f803f80u, 0x3f803f80u, 0x3f803f80u};
      const char* wb = smem + CV_W + (w & 2) * 8192 + (4 * g + q4) * 256 + (l & 3) * 8;
      float cs[4];
#pragma unroll
      for (int i = 0; i < 4; ++i) {
        const char* p = wb + (((4 * (w & 1) + i) ^ ((4 * g + q4) & 7)) * 32);
        cs[i] = MFMA16(__builtin_bit_cast(bf16x8, one4), lds_tr2(p + 8192, p + 12288),
                       MFMA16(__builtin_bit_cast(bf16x8, one4), lds_tr2(p, p + 4096), ((f32x4){0.f, 0.f, 0.f, 0.f})))[0];
        asm volatile("" : "+v"(cs[i]));
      }
      const float v = (g & 2) ? ((g & 1) ? cs[3] : cs[2]) : ((g & 1) ? cs[1] : cs[0]);
      st4(rw, b < 192 ? WS_CS + ((b & 15) * 3072 + (b >> 4) * 256 + t) * 4 : NOWHERE, __float_as_uint(v));
    }
    {
      const char* xs = smem + CV_P + 16384 + (32 * w + r) * 128 + (g & 1) * 8;
#pragma unroll
      for (int n = 0; n < 8; ++n) xa[n] = unpack4(*(const u32x2*)(xs + (n >> 2) * 2048 + (((2 * (n & 3) + (g >> 1)) ^ (r >> 1)) * 16)));
    }
    WAIT_VM(1);
    st4(rw, l == 0 ? WS_FY + (w * 256 + quad) * 16 : NOWHERE, tag);
    WAIT_VM(1);
    lds_barrier();
    if (slot == 1) code_warm(t);
    tag1 = tag;
   }
#pragma unroll 1
    for (int layer = 0; layer < 2; ++layer) {
      u32x2 x3[8];
      {
        PHASE_IDS
        f32x4 yf[8];
        auto rdy = [&]() {
#pragma unroll
          for (int n = 0; n < 8; ++n) {
            yf[n] = unpack4(*(const u32x2*)(smem + KO_Y + (4 * (n & 3) + g) * 1024 + (32 * w + 16 * ((n >> 2) ^ (g & 1)) + c) * 8));
            asm volatile("" : "+v"(yf[n]));
          }
        };
        float rs[2], nm[2];
        auto sums = [&](unsigned base) {
          u32x4 gv[4];
          poll([&]() {
            const unsigned po = opaque(base + panel * 16384 + 4 * g * 1024 + (16 * w + c) * 16);
#pragma unroll
            for (int i = 0; i < 4; ++i) gv[i] = ld16(rw, po + i * 1024);
            return (gv[0][1] & gv[0][3] & gv[1][1] & gv[1][3] & gv[2][1] & gv[2][3] & gv[3][1] & gv[3][3] & 1) != 0;
          });
          f32x4 sm = (__builtin_bit_cast(f32x4, gv[0]) + __builtin_bit_cast(f32x4, gv[1])) + (__builtin_bit_cast(f32x4, gv[2]) + __builtin_bit_cast(f32x4, gv[3]));
#pragma unroll
          for (int e = 0; e < 4; ++e) sm[e] = swap32_add(swap16_add(sm[e])) * (1.f / D_);
#pragma unroll
          for (int s = 0; s < 2; ++s) {
            rs[s] = __builtin_amdgcn_rsqf(fmaxf(sm[2 * s + 1] - sm[2 * s] * sm[2 * s], 0.f) + 1e-5f);
            nm[s] = -sm[2 * s] * rs[s];
            asm volatile("" : "+v"(rs[s]), "+v"(nm[s]));
          }
        };
        if (layer == 0) {
          sums(WS_EX0);
          lds_barrier();
          rdy();
        } else {
          rdy();
          __builtin_amdgcn_s_sleep(4);
          sums(WS_EX);
        }
        const unsigned go = (layer ? WS_X3 : WS_X1) + panel * 262144 + ct * 16384 + w * 2048 + l * 16;
#pragma unroll
        for (int s = 0; s < 2; ++s) {
#pragma unroll
          for (int n = 4 * s; n < 4 * s + 4; ++n) {
            const f32x4 v = xa[n] + (xa[n] * rs[s] + nm[s]) * yf[n];
            x3[n] = (u32x2){pk_bf(v[0], v[1]), pk_bf(v[2], v[3])};
            if (n & 1) st16(rw, go + ((n >> 1) & 1) * 8192 + s * 1024, (u32x4){x3[n - 1][0], x3[n - 1][1], x3[n][0], x3[n][1]});
          }
        }
        __builtin_amdgcn_s_barrier();
        SB0();
#pragma unroll
        for (int n = 1; n < 8; n += 2)
          *(u32x4*)(smem + ((n >> 1) & 1) * 8192 + (2 * w + (n >> 2)) * 1024 + l * 16) = (u32x4){x3[n - 1][0], x3[n - 1][1], x3[n][0], x3[n][1]};
        f32x4 xv[8];
#pragma unroll
        for (int n = 0; n < 8; ++n) xv[n] = unpack4(x3[n]);
        u32x4 u = row_sums(xv);
        asm volatile("" : "+v"(u));
        WAIT_VM(0);
        st16(rw, l < 16 ? rsoff + ct * 1024 + (16 * w + c) * 16 : NOWHERE, u);
      }
      {
        PHASE_IDS
        const int wr = w >> 1, wh = w & 1;
        f32x4 acc[6][4];
#pragma unroll
        for (int j = 0; j < 6; ++j)
#pragma unroll
          for (int i = 0; i < 4; ++i) { acc[j][i] = (f32x4){0.f, 0.f, 0.f, 0.f}; asm volatile("" : "+v"(acc[j][i])); }
        const lchar* qa = (lchar*)smem + wr * 4096 + l * 16;
        const lchar* qb = (lchar*)smem + 16384 + wh * 2048 + l * 16;
        bf16x8 fa[2][4], fb[2][6];
        auto rd = [&](int buf, int h, int r) {
          if (r == 1) fb[buf][0] = *(const volatile LAS bf16x8*)(qb + h * 12288);
          else if (r < 5) fa[buf][r ? r - 1 : 0] = *(const volatile LAS bf16x8*)(qa + h * 8192 + (r ? r - 1 : 0) * 1024);
          else fb[buf][r - 4] = *(const volatile LAS bf16x8*)(qb + h * 12288 + ((r - 4) >> 1) * 4096 + ((r - 4) & 1) * 1024);
        };
        auto mma = [&](int buf, int m0, int m1, int h = -1) {
#pragma unroll
          for (int m = m0; m < m1; ++m) {
            const int sn = (m & 4) ? 3 - (m & 3) : m & 3;
            const int j = m < 8 ? m >> 2 : 2 + sn, i = m < 8 ? sn : (m >> 2) - 2;
            if (j < 2) mfma_ip(acc[j][i], fb[buf][j], fa[buf][i]);
            else mfma_ip(acc[j][i], fa[buf][i], fb[buf][j]);
            if (h >= 0 && m - m0 < 10) rd(buf ^ 1, h, m - m0);
          }
        };
        u32x4 one4 = {0x3f803f80u, 0x3f803f80u, 0x3f803f80u, 0x3f803f80u};
        const f32x4 zero4 = {0.f, 0.f, 0.f, 0.f};
        asm volatile("" : "+v"(qa), "+v"(qb), "+v"(one4));
        int sl = 0;
        auto tile = [&]() {
          mma(0, 0, 16, 1);
          const int d = sl == 3 ? -3 * QK_STAGE : QK_STAGE;
          sl = (sl + 1) & 3;
          qa += d;
          asm volatile("" : "+v"(qa));
          mma(0, 16, 18);
          qb += d;
          asm volatile("" : "+v"(qb));
          mma(0, 18, 23);
          __builtin_amdgcn_s_waitcnt(0xC07F);
          mma(0, 23, 24);
          mma(1, 0, 1);
          lds_barrier();
          mma(1, 1, 23, 0);
          __builtin_amdgcn_s_waitcnt(0xC07F);
          mma(1, 23, 24);
        };
        lds_barrier();
#pragma unroll
        for (int r = 0; r < 10; ++r) rd(0, 0, r);
        __builtin_amdgcn_s_waitcnt(0xC07F);
        mma(0, 0, 23, 1);
        __builtin_amdgcn_s_waitcnt(0xC07F);
        mma(0, 23, 24);
        sl = 1; qa += QK_STAGE; qb += QK_STAGE;
        asm volatile("" : "+v"(qa), "+v"(qb));
        mma(1, 0, 16);
        lds_barrier();
        mma(1, 16, 24, 0);
        rd(0, 0, 8); rd(0, 0, 9);
        __builtin_amdgcn_s_waitcnt(0xC07F);
#pragma unroll 1
        for (int i = 1; i < 15; ++i) tile();
        mma(0, 8, 24, 1);
        u32x4 pqv[4];
        const unsigned pqo = WS_PQ + (unsigned)((panel * 32 + 2 * ct + wh) * 8 + 4 * wr) * 1024u + l * 16;
        auto qpart = [&]() {
          const f32x4 cq0 = *(const f32x4*)(s_cs + wh * 32 + 4 * g), cq1 = *(const f32x4*)(s_cs + wh * 32 + 16 + 4 * g);
#pragma unroll
          for (int i = 0; i < 4; ++i) {
            const float rs = s_rs[64 * wr + 16 * i + c], nm = s_rs[128 + 64 * wr + 16 * i + c];
            pqv[i] = __builtin_bit_cast(u32x4, pack8(phi4(acc[0][i] * rs + (nm * cq0 + 1e-6f * LOG2E)),
                                                     phi4(acc[1][i] * rs + (nm * cq1 + 1e-6f * LOG2E))));
          }
        };
        mma(1, 8, 14);
        lds_barrier();
        f32x4 rs0[2], nm0[2];
        float csk[2], csv[2];
#pragma unroll
        for (int h = 0; h < 2; ++h) {
          rs0[h] = *(const volatile LAS f32x4*)((LAS float*)s_rs + 64 * wr + 16 * h + 4 * g);
          nm0[h] = *(const volatile LAS f32x4*)((LAS float*)s_rs + 128 + 64 * wr + 16 * h + 4 * g);
          csk[h] = *(const volatile LAS float*)((LAS float*)s_cs + 64 + wh * 32 + 16 * h + c);
          csv[h] = *(const volatile LAS float*)((LAS float*)s_cs + 128 + wh * 32 + 16 * h + c);
        }
        mma(1, 14, 24);
        MFMA_DONE();
        {
          f32x4 kv[2][2], zz[2];
#pragma unroll
          for (int a = 0; a < 2; ++a) kv[a][0] = kv[a][1] = zz[a] = (f32x4){0.f, 0.f, 0.f, 0.f};
#pragma unroll
          for (int ip = 0; ip < 2; ++ip) {
            f32x4 rs[2], nm[2];
#pragma unroll
            for (int h = 0; h < 2; ++h) {
              rs[h] = ip ? *(const f32x4*)(s_rs + 64 * wr + 32 + 16 * h + 4 * g) : rs0[h];
              nm[h] = ip ? *(const f32x4*)(s_rs + 128 + 64 * wr + 32 + 16 * h + 4 * g) : nm0[h];
            }
            bf16x8 fk[2], fv[2];
#pragma unroll
            for (int a = 0; a < 2; ++a) {
              fk[a] = pack8(phi4(acc[2 + a][2 * ip] * rs[0] + (nm[0] * csk[a] + 1e-6f * LOG2E)),
                            phi4(acc[2 + a][2 * ip + 1] * rs[1] + (nm[1] * csk[a] + 1e-6f * LOG2E)));
            }
#pragma unroll
            for (int a = 0; a < 2; ++a)
              fv[a] = pack8(acc[4 + a][2 * ip] * rs[0] + nm[0] * csv[a], acc[4 + a][2 * ip + 1] * rs[1] + nm[1] * csv[a]);
#pragma unroll
            for (int a = 0; a < 2; ++a) {
              kv[a][0] = MFMA16(fk[a], fv[0], kv[a][0]);
              kv[a][1] = MFMA16(fk[a], fv[1], kv[a][1]);
              zz[a] = MFMA16(fk[a], __builtin_bit_cast(bf16x8, one4), zz[a]);
            }
          }
          float* dst = s_kv + wr * ((QK_KV2 - QK_KV) / 4);
#pragma unroll
          for (int qd = 0; qd < 4; ++qd) *(f32x4*)(dst + wh * 1024 + (qd >> 1) * 512 + l * 8 + (qd & 1) * 4) = kv[qd & 1][qd >> 1] * LN2;
          if (c == 0) { *(f32x4*)(dst + 2048 + wh * 32 + g * 8) = zz[0]; *(f32x4*)(dst + 2048 + wh * 32 + g * 8 + 4) = zz[1]; }
        }
        lds_barrier();
        __builtin_amdgcn_s_sleep(5);
        mma(0, 0, 8);
        mma(1, 0, 8);
        MFMA_DONE();
        qpart();
        asm volatile("" : "+v"(pqv[0]), "+v"(pqv[1]), "+v"(pqv[2]), "+v"(pqv[3]));
        lds_barrier();
        {
          const bf16x8 fk0 = lds_b128(smem + QK_KF + wh * 2048 + l * 16), fk1 = lds_b128(smem + QK_KF + wh * 2048 + 1024 + l * 16);
          const bf16x8 fz = lds_b128(smem + QK_KF + 4096 + wh * 64 + g * 16);
          f32x4 den[4], n0[4], n1[4];
#pragma unroll
          for (int i = 0; i < 4; ++i) {
            const bf16x8 fq = __builtin_bit_cast(bf16x8, pqv[i]);
            den[i] = MFMA16(fz, fq, ((f32x4){1e-6f, 1e-6f, 1e-6f, 1e-6f}));
            n0[i] = MFMA16(fk0, fq, zero4);
            n1[i] = MFMA16(fk1, fq, zero4);
          }
          SB0();
#pragma unroll
          for (int i = 0; i < 4; ++i) {
            const float inv = __builtin_amdgcn_rcpf(den[i][0]);
            st16(rw, pqo + i * 1024, __builtin_bit_cast(u32x4, pack8(n0[i] * inv, n1[i] * inv)));
          }
          asm volatile("" :: "v"(den[0]), "v"(den[1]), "v"(den[2]), "v"(den[3]));
        }
        lds_barrier();
      }
      {
        PHASE_IDS
#pragma unroll
        for (int n = 0; n < 8; ++n) { xa[n] = unpack4(x3[n]); asm volatile("" : "+v"(xa[n])); }
        const lchar* pw0 = (lchar*)smem + KO_R + l * 16;
        const lchar* pw1 = pw0 + 65536;
        unsigned vo = WS_PQ + panel * (32 * 8192) + w * 2048 + l * 16;
        asm volatile("" : "+v"(pw0), "+v"(pw1), "+v"(vo));
        bf16x8 fw[2][4];
        u32x4 fo[32][2];
        auto rdw = [&](int hd, int n) {
          const int sl = ((hd >> 2) + 2) & 7;
          fw[hd & 1][n] = *(const volatile LAS bf16x8*)((sl < 4 ? pw0 : pw1) + (sl & 3) * 16384 + (hd & 3) * 4096 + n * 1024);
        };
        auto ldo = [&](int hd) {
#pragma unroll
          for (int s = 0; s < 2; ++s) fo[hd][s] = ld16(rw, vo + s * 1024, hd * 8192);
        };
#pragma unroll
        for (int n = 0; n < 4; ++n) rdw(0, n);
        WAIT_VM(0);
        st4(rw, l == 0 ? WS_FQ + (((layer * 16 + panel) * 2 + (w >> 1)) * 32 + ct * 2 + (w & 1)) * 16 : NOWHERE, 1u);
        __builtin_amdgcn_s_sleep(18);
        poll([&]() { return ld4(rw, opaque(WS_FQ + (((layer * 16 + panel) * 2 + (w >> 1)) * 32 + (l & 31)) * 16)) != 0; });
#pragma unroll
        for (int hd = 0; hd < 6; ++hd) ldo(hd);
        SB0();
#pragma unroll
        for (int hd = 0; hd < 32; ++hd) {
          if (hd + 6 < 32) ldo(hd + 6);
          SB0();
          if (hd == 23) __builtin_amdgcn_s_barrier();
#pragma unroll
          for (int n = 0; n < 8; ++n) {
            mfma_ip(xa[n], fw[hd & 1][n & 3], __builtin_bit_cast(bf16x8, fo[hd][n >> 2]));
            if (hd < 31 && n < 4) rdw(hd + 1, n);
          }
          SB0();
        }
        MFMA_DONE();
        if (layer) {
          char* tb = smem + w * 4096;
#pragma unroll
          for (int n = 0; n < 8; ++n)
            *(u32x2*)(tb + (n >> 2) * 2048 + c * 128 + (((2 * (n & 3) + (g >> 1)) ^ (c >> 1)) * 16) + (g & 1) * 8) =
                (u32x2){pk_bf(xa[n][0], xa[n][1]), pk_bf(xa[n][2], xa[n][3])};
          const rsrc_t ro = make_rsrc(out, S_ * D_ * 2);
          const unsigned o = (panel * 128 + 32 * w + (l >> 3)) * 2048 + ct * 128 + (l & 7) * 16;
#pragma unroll
          for (int k = 0; k < 4; ++k)
            st16(ro, o + k * 16384, *(const u32x4*)(tb + (8 * k + (l >> 3)) * 128 + (((l & 7) ^ ((4 * k + (l >> 4)) & 7)) * 16)));
        } else {
          st16(rw, l < 16 ? WS_EX + panel * 16384 + ct * 1024 + (16 * w + c) * 16 : NOWHERE, row_sums(xa));
        }
      }
    }
    if (blockIdx.x == 0 && threadIdx.x == 0) st4(rw, WS_NONCE, tag1);
  } else {
    unsigned tag, f1;
    u32x4 yv[4];
    float csum = 0.f;
   {
    const int b = blockIdx.x, w = w0, l = threadIdx.x & 63;
    const int xcd = b & 7, slot = b >> 3, t = threadIdx.x & 255;
    const int quad = xcd * 32 + slot, ch = quad * 4 + (w & 3);
    char* cb = smem + (w & 3) * CV_CH;
    const u32x4 z4 = {0, 0, 0, 0};
    const int hw = w - 4, c16 = l & 15, g = l >> 4;
    const int panel = 8 * (xcd & 1) + (slot & 7), ct = 4 * (xcd >> 1) + (slot >> 3);
    const rsrc_t rx = make_rsrc(x0, S_ * D_ * 2), rh = make_rsrc(hk, S_ * D_ * 2);
    u32x4 xr[4], c[4];
    if (slot == 0) code_warm(t);
    else __builtin_amdgcn_s_sleep(36);
#pragma unroll
    for (int i = 0; i < 4; ++i) c[i] = __builtin_amdgcn_raw_buffer_load_b128(rh, ch * (S_ * 2) + l * 16 + i * 1024, 0, NT);
#pragma unroll
    for (int i = 0; i < 4; ++i)
      xr[i] = __builtin_amdgcn_raw_buffer_load_b128(rx, (panel * 128 + 32 * hw + 8 * i + (l >> 3)) * (D_ * 2) + ct * 128 + (l & 7) * 16, 0, NT);
    tag = ld4(rw, WS_NONCE);
    const bool isq = b < 192;
    const int ks = (b & 15) * 2 + (hw & 1), cgp = ((isq ? b : b - 192) >> 4) * 4 + (hw & 2);
    const unsigned pitch = isq ? 6144 : 2048;
    const rsrc_t rwt = make_rsrc(isq ? wqkv : wo, pitch * 1024);
    unsigned vo = (ks * 32 + g) * pitch + cgp * 128 + ((c16 ^ (2 * g)) * 16);
    SB0();
    lds_barrier();
    SB0();
    if (l < 4) *(u32x4*)(cb + l * 16) = z4;
    if (l < 3) *(u32x4*)(cb + CV_H + l * 16) = z4;
    if (l == 0) *(u32x4*)(cb + CV_H + 48) = (u32x4){0, 0, 0, c[0][0]};
#pragma unroll
    for (int i = 0; i < 4; ++i) {
      const int cc = l + 64 * i;
      *(u32x4*)(cb + 64 + cc * 16) = c[i];
      *(u32x4*)(cb + CV_H + 64 + cc * 16) = (u32x4){c[i][1], c[i][2], c[i][3], next_dword(c, i)};
    }
    tag = next_tag(tag);
    {
      float t1[4], t2[4];
#pragma unroll
      for (int i = 0; i < 4; ++i) {
        *(u32x4*)(smem + CV_P + 16384 + (32 * hw + 8 * i + (l >> 3)) * 128 + (((l & 7) ^ ((4 * i + g) & 7)) * 16)) = xr[i];
        f32x2 s1 = {0.f, 0.f}, s2 = s1;
#pragma unroll
        for (int j = 0; j < 4; ++j) {
          const f32x2 v = {bf_lo(xr[i][j]), bf_hi(xr[i][j])};
          s1 += v;
          s2 += v * v;
        }
        t1[i] = s1[0] + s1[1]; t2[i] = s2[0] + s2[1];
        t1[i] += dpp_mov<0xB1>(t1[i]); t2[i] += dpp_mov<0xB1>(t2[i]);
        t1[i] += dpp_mov<0x4E>(t1[i]); t2[i] += dpp_mov<0x4E>(t2[i]);
        t1[i] += dpp_mov<0x141>(t1[i]); t2[i] += dpp_mov<0x141>(t2[i]);
        asm volatile("" : "+v"(t1[i]), "+v"(t2[i]));
      }
      const float a1 = (l & 2) ? ((l & 1) ? t1[3] : t1[2]) : ((l & 1) ? t1[1] : t1[0]);
      const float a2 = (l & 2) ? ((l & 1) ? t2[3] : t2[2]) : ((l & 1) ? t2[1] : t2[0]);
      st8(rw, (l & 4) ? NOWHERE : WS_EX0 + panel * 16384 + ct * 1024 + (16 * hw + 8 * (l & 1) + (l >> 3)) * 16 + (l & 2) * 4,
              (u32x2){__float_as_uint(a1), __float_as_uint(a2) | 1u});
    }
    WAIT_VM(0);
    st4(rw, l == 0 ? WS_FX + ((panel * 4 + hw) * 16 + ct) * 16 : NOWHERE, tag);
    lds_barrier();
    SB0();
#pragma unroll
    for (int i = 0; i < 8; ++i)
      blds_n<1, 1>(rwt, (i & 1) ? vo ^ 128u : vo, i * 4 * pitch, (unsigned)(size_t)(LAS char*)smem + CV_W + hw * 8192 + i * 1024);
    poll([&]() { return ld4(rw, opaque(WS_FX + ((panel * 4 + hw) * 16 + c16) * 16)) == tag; });
    __builtin_amdgcn_s_sleep(12);
    {
      const char* wb = smem + CV_W + hw * 8192;
      const int q4 = (l >> 2) & 3;
      const int rd = (4 * g + q4) * 256 + (l & 3) * 8, fr = ((4 * g + q4) & 7) * 32;
      const unsigned o = (isq ? WS_WQ + (unsigned)((cgp & 15) * 32 + ks) * 12288u + (cgp >> 4) * 4096u
                              : WS_WO + (unsigned)(cgp * 32 + ks) * 4096u) + l * 16;
      const unsigned o2 = isq ? 32u * 12288u : 32u * 4096u;
#pragma unroll
      for (int i = 0; i < 8; ++i) {
        const char* p = wb + rd + ((i * 32) ^ fr);
        st16(rw, o + (i >> 2) * o2 + (i & 3) * 1024, __builtin_bit_cast(u32x4, lds_tr2(p, p + 4096)));
      }
      st16(rw, t < 200 ? WS_ZERO + (b * 200 + t) * 16 : NOWHERE, z4);
#pragma unroll
      for (int i = 0; i < 3; ++i) st16(rw, t + 256 * i < 528 ? WS_KVP + (b * 528 + t + 256 * i) * 16 : NOWHERE, z4);
    }
    lds_barrier();
    WAIT_VM(0);
    __builtin_amdgcn_s_barrier();
    if (hw == 0) st4(rw, l < 8 ? WS_F1 + (l * 256 + b) * 16 : NOWHERE, tag);
    __builtin_amdgcn_s_sleep(12);
    poll([&]() { return ld4(rw, opaque(WS_FY + ((panel & 3) * 256 + ct * 16 + 4 * hw + (l & 3)) * 16)) == tag; });
#pragma unroll
    for (int i = 0; i < 4; ++i)
      yv[i] = ld16(rw, WS_Y + panel * 262144 + (ct * 16 + 4 * hw + i) * 1024 + l * 16);
    f1 = ld4(rw, WS_F1 + (xcd * 256 + t) * 16);
    SB0();
#pragma unroll
    for (int i = 0; i < 4; ++i) *(u32x4*)(smem + KO_Y + (4 * hw + i) * 1024 + l * 16) = yv[i];
   }
#pragma unroll 1
    for (int layer = 0; layer < 2; ++layer) {
      PHASE_IDS
      float lnv;
      {
        if (layer == 0) {
          lds_barrier();
          if (!__all(f1 == tag))
            poll([&]() { return ld4(rw, opaque(WS_F1 + (xcd * 256 + t) * 16)) == tag; });
        }
        __builtin_amdgcn_s_barrier();
#pragma unroll
        for (int i = 0; i < 6; ++i) issue_b(i >> 1, i >> 1, i & 1);
        WAIT_VM(12);
        __builtin_amdgcn_s_barrier();
        u32x4 rr[4];
        if (layer == 0) {
          const unsigned co = t < 192 ? WS_CS + (4 * g * 3072 + lw * 1024 + ct * 64 + c * 4) * 4 : NOWHERE;
          f32x4 c0 = __builtin_bit_cast(f32x4, ld16(rw, co));
#pragma unroll
          for (int i = 1; i < 4; ++i) c0 += __builtin_bit_cast(f32x4, ld16(rw, co, i * 12288));
          auto q0 = __builtin_amdgcn_permlane16_swap(__float_as_uint(c0[0]), __float_as_uint(c0[1]), false, false);
          auto q1 = __builtin_amdgcn_permlane16_swap(__float_as_uint(c0[2]), __float_as_uint(c0[3]), false, false);
          auto q2 = __builtin_amdgcn_permlane32_swap(__float_as_uint(__uint_as_float(q0[0]) + __uint_as_float(q0[1])),
                                                     __float_as_uint(__uint_as_float(q1[0]) + __uint_as_float(q1[1])), false, false);
          csum = __uint_as_float(q2[0]) + __uint_as_float(q2[1]);
        } else {
          __builtin_amdgcn_s_sleep(4);
        }
        poll([&]() {
          const unsigned ro = opaque(rsoff + 4 * g * 1024 + (16 * lw + c) * 16);
#pragma unroll
          for (int i = 0; i < 4; ++i) rr[i] = ld16(rw, ro + i * 1024);
          return (rr[0][1] & rr[0][3] & rr[1][1] & rr[1][3] & rr[2][1] & rr[2][3] & rr[3][1] & rr[3][3] & 1) != 0;
        });
#pragma unroll
        for (int i = 2; i < 6; ++i) issue_a(i >> 1, i >> 1, i & 1);
        issue(3, 3, 0);
        lnv = ln_pair((__builtin_bit_cast(f32x4, rr[0]) + __builtin_bit_cast(f32x4, rr[1])) + (__builtin_bit_cast(f32x4, rr[2]) + __builtin_bit_cast(f32x4, rr[3])), g);
        asm volatile("" : "+v"(lnv));
        WAIT_VM(9);
        __builtin_amdgcn_s_barrier();
        issue(3, 3, 1); issue(4, 0, 0);
        WAIT_VM(15);
      }
      __builtin_amdgcn_s_barrier();
      for (int u = 2; u < 13; ++u) {
        issue(u + 2, (u + 2) & 3, 1); issue(u + 3, (u + 3) & 3, 0);
        WAIT_VM(15);
        __builtin_amdgcn_s_barrier();
      }
      issue(15, 3, 1);
      WAIT_VM(10);
      __builtin_amdgcn_s_barrier();
      stage_w(0);
      WAIT_VM(4);
      __builtin_amdgcn_s_barrier();
      s_cs[lw * 64 + c * 4 + g] = csum;
      s_rs[(g >> 1) * 128 + 32 * lw + 16 * (g & 1) + c] = lnv;
      stage_w(1); stage_w(2);
#pragma unroll
      for (int i = 0; i < 4; ++i) *(u32x4*)(smem + KO_Y + (4 * lw + i) * 1024 + l * 16) = yv[i];
      lds_barrier();
      if (layer == 0 && slot == 2) code_warm(t);
#pragma unroll
      for (int u = 3; u < 6; ++u) stage_w(u);
      __builtin_amdgcn_s_setprio(2);
      lds_barrier();
#pragma unroll
      for (int i = 0; i < 2; ++i) {
        const int e = i ? 256 + (t & 7) : t;
        const float* p = s_kv + e * 8;
        const u32x2 lo = pack_nz(*(const f32x4*)p + *(const f32x4*)(p + 2112)), hi = pack_nz(*(const f32x4*)(p + 4) + *(const f32x4*)(p + 2116));
        st16(rw, (i == 0 || t < 8) ? pb + panel * KVP_BLK + e * 16 : NOWHERE, (u32x4){lo[0], lo[1], hi[0], hi[1]});
      }
      kv_reduce(rw, pb, lw, l, smem);
      lds_barrier();
      __builtin_amdgcn_s_barrier();
#pragma unroll
      for (int u = 6; u < 8; ++u) stage_w(u);
      __builtin_amdgcn_s_setprio(0);
      WAIT_VM(0);
      __builtin_amdgcn_s_barrier();
    }
  }
}

extern "C" void kernel_launch(void* const* d_in, const int* in_sizes, int n_in, void* d_out, int out_size, void* d_ws,
                              size_t ws_size, hipStream_t stream) {
  const u16* x0 = (const u16*)d_in[0];
  const u16* kin = (const u16*)d_in[1];
  const u16* vin = (const u16*)d_in[2];
  const u16* sk = (const u16*)d_in[3];
  const u16* hk = (const u16*)d_in[4];
  const u16* dv = (const u16*)d_in[5];
  const u16* wqkv = (const u16*)d_in[7];
  const u16* wo = (const u16*)d_in[8];
  char* ws = (char*)d_ws;
  (void)hipFuncSetAttribute((const void*)k_all, hipFuncAttributeMaxDynamicSharedMemorySize, QK_LDS);
  k_all<<<256, 512, QK_LDS, stream>>>(x0, kin, vin, sk, hk, dv, wqkv, wo, ws, (char*)d_out);
}
```
